```python
import torch

def _softmax(x, axis=-1):
    return torch.softmax(x, dim=axis)
def _gelu(x):
    return torch.nn.functional.gelu(x, approximate="tanh")
def _silu(x):
    return torch.nn.functional.silu(x)
def _sigmoid(x):
    return torch.sigmoid(x)
def _layernorm(x, axis=-1, eps=1e-5):
    m = x.mean(dim=axis, keepdim=True)
    v = x.var(dim=axis, keepdim=True, unbiased=False)
    return (x - m) / torch.sqrt(v + eps)
def _scatter_add(tgt, idx, src, axis=0):
    return tgt.clone().index_add_(axis, idx, src)
def _segment_sum(src, seg, n):
    out = torch.zeros((n,) + src.shape[1:], dtype=src.dtype)
    return out.index_add_(0, seg, src)
def _pad(x, pw):
    flat = tuple(v for pair in reversed(pw) for v in pair)
    return torch.nn.functional.pad(x, flat)

kernel_name = 'gated_mix__linear_attn_block__dense_activation_relu__prompt'

def setup_inputs():

    _w = (lambda a: (lambda t: t.to(torch.bfloat16) if t.is_floating_point() else t)(torch.as_tensor(a)))
    def _A_setup():
        B, S, D = 1, 2048, 640
        return {
            "x": _w((lambda *s: torch.randn(*s, dtype=torch.bfloat16) * 0.05)(B, S, D)),
            "wqkv": _w((lambda *s: torch.randn(*s, dtype=torch.bfloat16) * 0.8)(D, 3 * D)),
            "wo": _w((lambda *s: torch.randn(*s, dtype=torch.bfloat16) * 0.2)(D, D)),
        }
    _A_in = _A_setup()

    def _B_setup():
        B, S, D, Dff = 1, 2048, 640, 1536
        return {
            "x": _w((lambda *s: torch.randn(*s, dtype=torch.bfloat16) * 0.05)(B, S, D)),
            "w1": _w((lambda *s: torch.randn(*s, dtype=torch.bfloat16) * 0.05)(D, Dff)),
            "w2": _w((lambda *s: torch.randn(*s, dtype=torch.bfloat16) * 0.05)(Dff, D)),
        }
    _B_in = _B_setup()

    return {
        "A_x": _A_in["x"],
        "A_wqkv": _A_in["wqkv"],
        "A_wo": _A_in["wo"],
        "B_x": _B_in["x"],
        "B_w1": _B_in["w1"],
        "B_w2": _B_in["w2"],
        "gate_w": _w((lambda *s: torch.randn(*s, dtype=torch.bfloat16) * 0.05)(640) * 16.0),
    }

def reference(A_x, A_wqkv, A_wo, B_x, B_w1, B_w2, gate_w):
    def _A_ref(x, wqkv, wo):
        B, S, D = x.shape
        H = 32; Dh = D // H
        qkv = (lambda x, *d: x.permute(*d))((x @ wqkv).reshape(B, S, 3, H, Dh), 2, 0, 3, 1, 4)
        q, k, v = qkv[0], qkv[1], qkv[2]
        phi = lambda t: torch.where(t > 0, t + 1, torch.exp(t)) + 1e-6
        pq, pk = phi(q), phi(k)
        kv = torch.einsum("bhsd,bhse->bhde", pk, v)
        z = torch.einsum("bhsd->bhd", pk)
        num = torch.einsum("bhqd,bhde->bhqe", pq, kv)
        den = torch.einsum("bhqd,bhd->bhq", pq, z)[..., None] + 1e-6
        o = num / den
        return (lambda x, *d: x.permute(*d))(o, 0, 2, 1, 3).reshape(B, S, D) @ wo

    def _B_ref(x, w1, w2):
        h = torch.einsum("bsd,df->bsf", x, w1)
        zero = torch.zeros_like(h)
        h = torch.maximum(h, zero)
        return torch.einsum("bsf,fd->bsd", h, w2)

    x = A_x
    g = _sigmoid((x * gate_w).sum(-1, keepdims=True))
    a = _A_ref(x, A_wqkv, A_wo)
    b = _B_ref(x, B_w1, B_w2)
    return g * a + (1 - g) * b
```

```cpp
#include <hip/hip_runtime.h>
#include <hip/hip_bf16.h>
typedef __hip_bfloat16 bf16;
constexpr int S=2048, D=640, F=1536, H=32, DH=20;
__device__ __forceinline__ float bfr(float x){ return __bfloat162float(__float2bfloat16(x)); }
__device__ __forceinline__ float ld(const bf16* p){ return __bfloat162float(*p); }
__global__ void gemm_naive(const bf16* A, const bf16* B, bf16* C, int M, int N, int K, int mode){
  int n = blockIdx.x*64 + (threadIdx.x&63); int m = blockIdx.y*4 + (threadIdx.x>>6);
  if(n>=N||m>=M) return;
  float acc=0; for(int k=0;k<K;++k) acc += ld(A+(size_t)m*K+k)*ld(B+(size_t)k*N+n);
  if(mode==1) acc = fmaxf(bfr(acc),0.f);
  C[(size_t)m*N+n]=__float2bfloat16(acc);
}
__device__ __forceinline__ float phi(float t){ float r = t>0.f ? bfr(t+1.f) : bfr(__expf(t)); return bfr(r+1e-6f); }
__global__ void kv_kernel(const bf16* qkv, float* kv, float* z){
  int h=blockIdx.x; int t=threadIdx.x;
  if(t<400){ int d=t/20,e=t%20; float acc=0; for(int s=0;s<S;++s) acc+=phi(ld(qkv+(size_t)s*1920+640+h*20+d))*ld(qkv+(size_t)s*1920+1280+h*20+e); kv[h*400+t]=bfr(acc);}
  else if(t<420){ int d=t-400; float acc=0; for(int s=0;s<S;++s) acc+=phi(ld(qkv+(size_t)s*1920+640+h*20+d)); z[h*20+d]=bfr(acc);}
}
__global__ void o_kernel(const bf16* qkv, const float* kv, const float* z, bf16* o){
  int s=blockIdx.x; int c=threadIdx.x; int h=c/20,e=c%20;
  float num=0,den=0; for(int d=0;d<20;++d){ float p=phi(ld(qkv+(size_t)s*1920+h*20+d)); num+=p*kv[h*400+d*20+e]; den+=p*z[h*20+d]; }
  num=bfr(num); den=bfr(bfr(den)+1e-6f); o[(size_t)s*640+c]=__float2bfloat16(num/den);
}
__global__ void fin_kernel(const bf16* x, const bf16* gw, const bf16* a, const bf16* b, bf16* out){
  int s=blockIdx.x; int c=threadIdx.x; __shared__ float red[640];
  red[c]=bfr(ld(x+(size_t)s*640+c)*ld(gw+c)); __syncthreads();
  float t=0; for(int i=0;i<640;++i) t+=red[i]; t=bfr(t);
  float g=bfr(1.f/(1.f+__expf(-t))); float og=bfr(1.f-g);
  float r=bfr(g*ld(a+(size_t)s*640+c))+bfr(og*ld(b+(size_t)s*640+c));
  out[(size_t)s*640+c]=__float2bfloat16(r);
}
extern "C" void kernel_launch(void* const* d_in, const int* in_sizes, int n_in, void* d_out, int out_size, void* d_ws, size_t ws_size, hipStream_t stream){
  const bf16* x=(const bf16*)d_in[0]; const bf16* wqkv=(const bf16*)d_in[1]; const bf16* wo=(const bf16*)d_in[2];
  const bf16* w1=(const bf16*)d_in[4]; const bf16* w2=(const bf16*)d_in[5]; const bf16* gw=(const bf16*)d_in[6];
  char* ws=(char*)d_ws;
  bf16* qkv=(bf16*)ws; bf16* hh=(bf16*)(ws+(8<<20)); bf16* o=(bf16*)(ws+(16<<20)); bf16* a=(bf16*)(ws+(20<<20)); bf16* b=(bf16*)(ws+(24<<20));
  float* kv=(float*)(ws+(28<<20)); float* z=(float*)(ws+(29<<20));
  gemm_naive<<<dim3(30,512),256,0,stream>>>(x,wqkv,qkv,S,1920,640,0);
  gemm_naive<<<dim3(24,512),256,0,stream>>>(x,w1,hh,S,1536,640,1);
  kv_kernel<<<32,448,0,stream>>>(qkv,kv,z);
  o_kernel<<<S,640,0,stream>>>(qkv,kv,z,o);
  gemm_naive<<<dim3(10,512),256,0,stream>>>(o,wo,a,S,640,640,0);
  gemm_naive<<<dim3(10,512),256,0,stream>>>(hh,w2,b,S,640,1536,0);
  fin_kernel<<<S,640,0,stream>>>(x,gw,a,b,(bf16*)d_out);
}
```

```cpp
#include <hip/hip_runtime.h>
#include <hip/hip_bf16.h>

#define GAS __attribute__((address_space(1)))
#define LAS __attribute__((address_space(3)))
#define DI __device__ __forceinline__
typedef unsigned v4u __attribute__((ext_vector_type(4)));
typedef unsigned v2u __attribute__((ext_vector_type(2)));
typedef float f32x4 __attribute__((ext_vector_type(4)));
typedef float f32x2 __attribute__((ext_vector_type(2)));
typedef short s16x2 __attribute__((ext_vector_type(2)));
typedef short s16x4 __attribute__((ext_vector_type(4)));
typedef __bf16 bf16x2 __attribute__((ext_vector_type(2)));
typedef __bf16 bf16x8 __attribute__((ext_vector_type(8)));
typedef unsigned long long u64;
typedef __amdgpu_buffer_rsrc_t rsrc_t;

namespace {
constexpr int S = 2048, D = 640, FF = 1536;
constexpr int OFF_E = 0;
constexpr int OFF_PAD = 1024;
constexpr int OFF_F0 = 8192;
constexpr int OFF_F1 = OFF_F0 + 32768;
constexpr int OFF_F2 = OFF_F1 + 32768;
constexpr int OFF_G = OFF_F2 + 32768;
constexpr int OFF_KVP = OFF_G + 65536;
constexpr int KVP_STRIDE = 4096;
constexpr int OFF_B2 = 2 << 20;
constexpr int B2_STRIDE = 68 * 5120 + 128;
constexpr int OFF_A2 = 6 << 20;
constexpr int A2_STRIDE = 68 * 4096 + 128;
constexpr int WS_BYTES = 16 << 20;
constexpr int OFF_PROF = 16 << 20;
constexpr int STAGE = 45056, BOFF = 16384;
constexpr int L_SCR = 3 * STAGE;
constexpr int L_TAB = 148608;
constexpr int L_GATE = L_TAB + 13440;
constexpr int L_W = L_GATE + 1280;
constexpr int L_DUMP = L_W + 256;
constexpr int LDS_BYTES = L_DUMP + 256;
static_assert(LDS_BYTES == 163840 && L_W == 163328, "LDS map");
constexpr int AUX_SC1 = 16;
#define PT_DRAIN 2
#define F0_AT 9
#define PRIO 0
#define PT_SPLIT 0
#define SHADOW 3
#define XFIRST 0
#define SNAKE 1
#define CSH 0
#define TLBW 3
#define KVSLEEP 3
#define KVDELAY 2
#define EDELAY 48
#define OROT 0
#define HROT 0
#define AB10 16
#define PTSLEEP 0
#define GA 2
#define GB 4
#define ROTM 2
#define LATE_B10 1
#define RSL 1
constexpr int RING_SLEEP = RSL;
constexpr unsigned SPIN_CAP = 1u << 16;
constexpr u64 EPOCH_STEP = 0x9E3779B97F4A7C15ull;
constexpr int CODE_BYTES = 25984;

#define WG_BARRIER() do { __builtin_amdgcn_sched_barrier(0); asm volatile("s_waitcnt lgkmcnt(0)\n\ts_barrier" ::: "memory"); __builtin_amdgcn_sched_barrier(0); } while (0)
#define WAIT_VM(n) asm volatile("s_waitcnt vmcnt(" #n ")" ::: "memory")
#define FENCE() __builtin_amdgcn_sched_barrier(0)
#define PIN(x) asm volatile("" : "+v"(x))
#define RLX_AGENT __ATOMIC_RELAXED, __HIP_MEMORY_SCOPE_AGENT
#define RLX_WG __ATOMIC_RELAXED, __HIP_MEMORY_SCOPE_WORKGROUP

#define STAMP(i) do {} while (0)
#define STAMP_DECL
#define STAMP_OUT() do {} while (0)
#define LITE0() do {} while (0)
#define LITE1(l) do {} while (0)

#define N_EARLY 0
#define EARLY_LOADS(t, vo, rs, pos) do { \
} while (0)
#define KV_ASM \
  "s_nop 4\n\t" \
  "s_mov_b32 %[cnt], %[k0]\n\t" \
  "buffer_load_dwordx4 v[128:131], %[v0], %[rs], %[cnt] offen sc1\n\t" \
  "s_add_u32 %[cnt], %[cnt], 4096\n\t" \
  "s_and_b32 %[cnt], %[cnt], 0xffff\n\t" \
  "buffer_load_dwordx4 v[132:135], %[v0], %[rs], %[cnt] offen sc1\n\t" \
  "s_add_u32 %[cnt], %[cnt], 4096\n\t" \
  "s_and_b32 %[cnt], %[cnt], 0xffff\n\t" \
  "buffer_load_dwordx4 v[136:139], %[v0], %[rs], %[cnt] offen sc1\n\t" \
  "s_add_u32 %[cnt], %[cnt], 4096\n\t" \
  "s_and_b32 %[cnt], %[cnt], 0xffff\n\t" \
  "buffer_load_dwordx4 v[140:143], %[v0], %[rs], %[cnt] offen sc1\n\t" \
  "s_add_u32 %[cnt], %[cnt], 4096\n\t" \
  "s_and_b32 %[cnt], %[cnt], 0xffff\n\t" \
  "buffer_load_dwordx4 v[144:147], %[v0], %[rs], %[cnt] offen sc1\n\t" \
  "s_add_u32 %[cnt], %[cnt], 4096\n\t" \
  "s_and_b32 %[cnt], %[cnt], 0xffff\n\t" \
  "buffer_load_dwordx4 v[148:151], %[v0], %[rs], %[cnt] offen sc1\n\t" \
  "s_add_u32 %[cnt], %[cnt], 4096\n\t" \
  "s_and_b32 %[cnt], %[cnt], 0xffff\n\t" \
  "buffer_load_dwordx4 v[152:155], %[v0], %[rs], %[cnt] offen sc1\n\t" \
  "s_add_u32 %[cnt], %[cnt], 4096\n\t" \
  "s_and_b32 %[cnt], %[cnt], 0xffff\n\t" \
  "buffer_load_dwordx4 v[156:159], %[v0], %[rs], %[cnt] offen sc1\n\t" \
  "s_add_u32 %[cnt], %[cnt], 4096\n\t" \
  "s_and_b32 %[cnt], %[cnt], 0xffff\n\t" \
  "buffer_load_dwordx4 v[160:163], %[v0], %[rs], %[cnt] offen sc1\n\t" \
  "s_add_u32 %[cnt], %[cnt], 4096\n\t" \
  "s_and_b32 %[cnt], %[cnt], 0xffff\n\t" \
  "buffer_load_dwordx4 v[164:167], %[v0], %[rs], %[cnt] offen sc1\n\t" \
  "s_add_u32 %[cnt], %[cnt], 4096\n\t" \
  "s_and_b32 %[cnt], %[cnt], 0xffff\n\t" \
  "buffer_load_dwordx4 v[168:171], %[v0], %[rs], %[cnt] offen sc1\n\t" \
  "s_add_u32 %[cnt], %[cnt], 4096\n\t" \
  "s_and_b32 %[cnt], %[cnt], 0xffff\n\t" \
  "buffer_load_dwordx4 v[172:175], %[v0], %[rs], %[cnt] offen sc1\n\t" \
  "s_add_u32 %[cnt], %[cnt], 4096\n\t" \
  "s_and_b32 %[cnt], %[cnt], 0xffff\n\t" \
  "buffer_load_dwordx4 v[176:179], %[v0], %[rs], %[cnt] offen sc1\n\t" \
  "s_add_u32 %[cnt], %[cnt], 4096\n\t" \
  "s_and_b32 %[cnt], %[cnt], 0xffff\n\t" \
  "buffer_load_dwordx4 v[180:183], %[v0], %[rs], %[cnt] offen sc1\n\t" \
  "s_add_u32 %[cnt], %[cnt], 4096\n\t" \
  "s_and_b32 %[cnt], %[cnt], 0xffff\n\t" \
  "buffer_load_dwordx4 v[184:187], %[v0], %[rs], %[cnt] offen sc1\n\t" \
  "s_add_u32 %[cnt], %[cnt], 4096\n\t" \
  "s_and_b32 %[cnt], %[cnt], 0xffff\n\t" \
  "buffer_load_dwordx4 v[188:191], %[v0], %[rs], %[cnt] offen sc1\n\t" \
  "s_waitcnt vmcnt(15)\n\t" \
  "v_mfma_f32_16x16x32_bf16 %[sa], %[ia], v[128:131], 0\n\t" \
  "v_mfma_f32_16x16x32_bf16 %[sb], %[ib], v[128:131], 0\n\t" \
  "s_waitcnt vmcnt(14)\n\t" \
  "v_mfma_f32_16x16x32_bf16 %[sa], %[ia], v[132:135], %[sa]\n\t" \
  "v_mfma_f32_16x16x32_bf16 %[sb], %[ib], v[132:135], %[sb]\n\t" \
  "s_waitcnt vmcnt(13)\n\t" \
  "v_mfma_f32_16x16x32_bf16 %[sa], %[ia], v[136:139], %[sa]\n\t" \
  "v_mfma_f32_16x16x32_bf16 %[sb], %[ib], v[136:139], %[sb]\n\t" \
  "s_waitcnt vmcnt(12)\n\t" \
  "v_mfma_f32_16x16x32_bf16 %[sa], %[ia], v[140:143], %[sa]\n\t" \
  "v_mfma_f32_16x16x32_bf16 %[sb], %[ib], v[140:143], %[sb]\n\t" \
  "s_waitcnt vmcnt(11)\n\t" \
  "v_mfma_f32_16x16x32_bf16 %[sa], %[ia], v[144:147], %[sa]\n\t" \
  "v_mfma_f32_16x16x32_bf16 %[sb], %[ib], v[144:147], %[sb]\n\t" \
  "s_waitcnt vmcnt(10)\n\t" \
  "v_mfma_f32_16x16x32_bf16 %[sa], %[ia], v[148:151], %[sa]\n\t" \
  "v_mfma_f32_16x16x32_bf16 %[sb], %[ib], v[148:151], %[sb]\n\t" \
  "s_waitcnt vmcnt(9)\n\t" \
  "v_mfma_f32_16x16x32_bf16 %[sa], %[ia], v[152:155], %[sa]\n\t" \
  "v_mfma_f32_16x16x32_bf16 %[sb], %[ib], v[152:155], %[sb]\n\t" \
  "s_waitcnt vmcnt(8)\n\t" \
  "v_mfma_f32_16x16x32_bf16 %[sa], %[ia], v[156:159], %[sa]\n\t" \
  "v_mfma_f32_16x16x32_bf16 %[sb], %[ib], v[156:159], %[sb]\n\t" \
  "s_waitcnt vmcnt(7)\n\t" \
  "v_mfma_f32_16x16x32_bf16 %[sa], %[ia], v[160:163], %[sa]\n\t" \
  "v_mfma_f32_16x16x32_bf16 %[sb], %[ib], v[160:163], %[sb]\n\t" \
  "s_waitcnt vmcnt(6)\n\t" \
  "v_mfma_f32_16x16x32_bf16 %[sa], %[ia], v[164:167], %[sa]\n\t" \
  "v_mfma_f32_16x16x32_bf16 %[sb], %[ib], v[164:167], %[sb]\n\t" \
  "s_waitcnt vmcnt(5)\n\t" \
  "v_mfma_f32_16x16x32_bf16 %[sa], %[ia], v[168:171], %[sa]\n\t" \
  "v_mfma_f32_16x16x32_bf16 %[sb], %[ib], v[168:171], %[sb]\n\t" \
  "s_waitcnt vmcnt(4)\n\t" \
  "v_mfma_f32_16x16x32_bf16 %[sa], %[ia], v[172:175], %[sa]\n\t" \
  "v_mfma_f32_16x16x32_bf16 %[sb], %[ib], v[172:175], %[sb]\n\t" \
  "s_waitcnt vmcnt(3)\n\t" \
  "v_mfma_f32_16x16x32_bf16 %[sa], %[ia], v[176:179], %[sa]\n\t" \
  "v_mfma_f32_16x16x32_bf16 %[sb], %[ib], v[176:179], %[sb]\n\t" \
  "s_waitcnt vmcnt(2)\n\t" \
  "v_mfma_f32_16x16x32_bf16 %[sa], %[ia], v[180:183], %[sa]\n\t" \
  "v_mfma_f32_16x16x32_bf16 %[sb], %[ib], v[180:183], %[sb]\n\t" \
  "s_waitcnt vmcnt(1)\n\t" \
  "v_mfma_f32_16x16x32_bf16 %[sa], %[ia], v[184:187], %[sa]\n\t" \
  "v_mfma_f32_16x16x32_bf16 %[sb], %[ib], v[184:187], %[sb]\n\t" \
  "s_waitcnt vmcnt(0)\n\t" \
  "v_mfma_f32_16x16x32_bf16 %[sa], %[ia], v[188:191], %[sa]\n\t" \
  "v_mfma_f32_16x16x32_bf16 %[sb], %[ib], v[188:191], %[sb]\n\t" \
  "s_nop 7\n\t"
#define KV_CLOBBERS "v126", "v127", "v128", "v129", "v130", "v131", "v132", "v133", "v134", "v135", "v136", "v137", "v138", "v139", "v140", "v141", "v142", "v143", "v144", "v145", "v146", "v147", "v148", "v149", "v150", "v151", "v152", "v153", "v154", "v155", "v156", "v157", "v158", "v159", "v160", "v161", "v162", "v163", "v164", "v165", "v166", "v167", "v168", "v169", "v170", "v171", "v172", "v173", "v174", "v175", "v176", "v177", "v178", "v179", "v180", "v181", "v182", "v183", "v184", "v185", "v186", "v187", "v188", "v189", "v190", "v191"
#define P2_ASM \
  "s_nop 4\n\t" \
  "v_add_u32 v16, 0x1000, %[vo]\n\t" \
  "s_mul_i32 %[cnt], %[w], 2\n\t" \
  "s_add_u32 %[cnt], %[cnt], 0\n\t" \
  "s_add_u32 %[tb], %[cnt], %[r20]\n\t" \
  "s_sub_u32 %[ta], %[tb], 20\n\t" \
  "s_cselect_b32 %[tb], %[tb], %[ta]\n\t" \
  "s_mul_i32 %[tb], %[tb], 5120\n\t" \
  "s_add_u32 %[tb], %[tb], %[bO]\n\t" \
  "s_mul_i32 %[cnt], %[cnt], 5120\n\t" \
  "s_add_u32 %[cnt], %[cnt], 45056\n\t" \
  "s_add_u32 m0, %[cnt], %[l0]\n\t" \
  "s_nop 0\n\t" \
  "buffer_load_dwordx4 %[vo], %[rs], %[tb] offen offset:0 sc1 lds\n\t" \
  "s_mul_i32 %[rdy], %[w], 10240\n\t" \
  "s_add_u32 %[rdy], %[rdy], %[l0]\n\t" \
  "v_add_u32 v10, %[rdy], %[vo]\n\t" \
  "buffer_load_dwordx4 %[vo], %[rs], %[tb] offen offset:1024 sc1 lds\n\t" \
  "v_and_b32 v11, 16, %[vo]\n\t" \
  "v_lshlrev_b32 v11, 3, v11\n\t" \
  "s_lshl_b32 %[rdy], %[w], 10\n\t" \
  "buffer_load_dwordx4 %[vo], %[rs], %[tb] offen offset:2048 sc1 lds\n\t" \
  "v_add_u32 v12, %[rdy], %[vo]\n\t" \
  "v_xor_b32 v13, 1024, v12\n\t" \
  "v_xor_b32 v14, 2048, v12\n\t" \
  "buffer_load_dwordx4 %[vo], %[rs], %[tb] offen offset:3072 sc1 lds\n\t" \
  "v_xor_b32 v15, 3072, v12\n\t" \
  "s_add_u32 %[rdy], %[l0], 163344\n\t" \
  "v_mov_b32 v18, %[rdy]\n\t" \
  "s_add_u32 m0, m0, 4096\n\t" \
  "s_nop 0\n\t" \
  "buffer_load_dwordx4 v16, %[rs], %[tb] offen offset:0 sc1 lds\n\t" \
  "v_mov_b32 v19, 1\n\t" \
  "v_bfe_u32 v17, %[vo], 4, 4\n\t" \
  "v_lshlrev_b32 v17, 2, v17\n\t" \
  "s_waitcnt vmcnt(5)\n\t" \
  "s_barrier\n\t" \
  "s_mul_i32 %[cnt], %[w], 2\n\t" \
  "s_add_u32 %[cnt], %[cnt], 1\n\t" \
  "s_add_u32 %[tb], %[cnt], %[r20]\n\t" \
  "s_sub_u32 %[ta], %[tb], 20\n\t" \
  "s_cselect_b32 %[tb], %[tb], %[ta]\n\t" \
  "s_mul_i32 %[tb], %[tb], 5120\n\t" \
  "s_add_u32 %[tb], %[tb], %[bO]\n\t" \
  "s_mul_i32 %[cnt], %[cnt], 5120\n\t" \
  "s_add_u32 %[cnt], %[cnt], 45056\n\t" \
  "s_add_u32 m0, %[cnt], %[l0]\n\t" \
  "s_nop 0\n\t" \
  "buffer_load_dwordx4 %[vo], %[rs], %[tb] offen offset:0 sc1 lds\n\t" \
  "s_add_u32 s60, %[rot], 0\n\t" \
  "s_and_b32 s60, s60, 7\n\t" \
  "s_mul_i32 s60, s60, 4096\n\t" \
  "s_waitcnt vmcnt(5)\n\t" \
  "buffer_load_dwordx4 %[vo], %[rs], %[tb] offen offset:1024 sc1 lds\n\t" \
  "s_add_u32 s60, s60, %[aB]\n\t" \
  "s_add_u32 s61, %[rot], 1\n\t" \
  "s_and_b32 s61, s61, 7\n\t" \
  "s_waitcnt vmcnt(5)\n\t" \
  "buffer_load_dwordx4 %[vo], %[rs], %[tb] offen offset:2048 sc1 lds\n\t" \
  "s_mul_i32 s61, s61, 4096\n\t" \
  "s_add_u32 s61, s61, %[aB]\n\t" \
  "s_add_u32 s62, %[rot], 2\n\t" \
  "s_waitcnt vmcnt(5)\n\t" \
  "buffer_load_dwordx4 %[vo], %[rs], %[tb] offen offset:3072 sc1 lds\n\t" \
  "s_and_b32 s62, s62, 7\n\t" \
  "s_mul_i32 s62, s62, 4096\n\t" \
  "s_add_u32 s62, s62, %[aB]\n\t" \
  "s_waitcnt vmcnt(5)\n\t" \
  "s_add_u32 m0, m0, 4096\n\t" \
  "s_nop 0\n\t" \
  "buffer_load_dwordx4 v16, %[rs], %[tb] offen offset:0 sc1 lds\n\t" \
  "s_add_u32 s63, %[rot], 3\n\t" \
  "s_and_b32 s63, s63, 7\n\t" \
  "s_mul_i32 s63, s63, 4096\n\t" \
  "s_waitcnt vmcnt(5)\n\t" \
  "s_mul_i32 %[cnt], %[w], 3\n\t" \
  "s_add_u32 %[cnt], %[cnt], 8\n\t" \
  "s_add_u32 %[tb], %[cnt], %[r20]\n\t" \
  "s_sub_u32 %[ta], %[tb], 20\n\t" \
  "s_cselect_b32 %[tb], %[tb], %[ta]\n\t" \
  "s_mul_i32 %[tb], %[tb], 5120\n\t" \
  "s_add_u32 %[tb], %[tb], %[bO]\n\t" \
  "s_mul_i32 %[cnt], %[cnt], 5120\n\t" \
  "s_add_u32 %[cnt], %[cnt], 45056\n\t" \
  "s_add_u32 m0, %[cnt], %[l0]\n\t" \
  "s_nop 0\n\t" \
  "buffer_load_dwordx4 %[vo], %[rs], %[tb] offen offset:0 sc1 lds\n\t" \
  "s_add_u32 s63, s63, %[aB]\n\t" \
  "s_add_u32 s64, %[rot], 4\n\t" \
  "s_and_b32 s64, s64, 7\n\t" \
  "s_waitcnt vmcnt(5)\n\t" \
  "buffer_load_dwordx4 %[vo], %[rs], %[tb] offen offset:1024 sc1 lds\n\t" \
  "s_mul_i32 s64, s64, 4096\n\t" \
  "s_add_u32 s64, s64, %[aB]\n\t" \
  "s_add_u32 s65, %[rot], 5\n\t" \
  "s_waitcnt vmcnt(5)\n\t" \
  "buffer_load_dwordx4 %[vo], %[rs], %[tb] offen offset:2048 sc1 lds\n\t" \
  "s_and_b32 s65, s65, 7\n\t" \
  "s_mul_i32 s65, s65, 4096\n\t" \
  "s_add_u32 s65, s65, %[aB]\n\t" \
  "s_waitcnt vmcnt(5)\n\t" \
  "buffer_load_dwordx4 %[vo], %[rs], %[tb] offen offset:3072 sc1 lds\n\t" \
  "s_add_u32 s66, %[rot], 6\n\t" \
  "s_and_b32 s66, s66, 7\n\t" \
  "s_mul_i32 s66, s66, 4096\n\t" \
  "s_waitcnt vmcnt(5)\n\t" \
  "s_add_u32 m0, m0, 4096\n\t" \
  "s_nop 0\n\t" \
  "buffer_load_dwordx4 v16, %[rs], %[tb] offen offset:0 sc1 lds\n\t" \
  "s_add_u32 s66, s66, %[aB]\n\t" \
  "s_add_u32 s67, %[rot], 7\n\t" \
  "s_and_b32 s67, s67, 7\n\t" \
  "s_waitcnt vmcnt(5)\n\t" \
  "s_barrier\n\t" \
  "s_add_u32 %[tb], %[rot], 0\n\t" \
  "s_and_b32 %[tb], %[tb], 7\n\t" \
  "s_mul_i32 %[tb], %[tb], 5120\n\t" \
  "s_add_u32 %[tb], %[tb], %[bB]\n\t" \
  "buffer_load_dwordx4 v[176:179], %[vo], %[rs], %[tb] offen offset:0 sc1\n\t" \
  "s_mul_i32 s67, s67, 4096\n\t" \
  "s_add_u32 s67, s67, %[aB]\n\t" \
  "s_add_u32 s68, %[rot], 0\n\t" \
  "s_waitcnt vmcnt(5)\n\t" \
  "buffer_load_dwordx4 v[180:183], %[vo], %[rs], %[tb] offen offset:1024 sc1\n\t" \
  "s_bfe_u32 s68, s68, 0x20001\n\t" \
  "s_mul_i32 s68, s68, 4096\n\t" \
  "s_add_u32 s68, s68, %[aA]\n\t" \
  "s_waitcnt vmcnt(5)\n\t" \
  "buffer_load_dwordx4 v[184:187], %[vo], %[rs], %[tb] offen offset:2048 sc1\n\t" \
  "s_add_u32 s69, %[rot], 2\n\t" \
  "s_bfe_u32 s69, s69, 0x20001\n\t" \
  "s_mul_i32 s69, s69, 4096\n\t" \
  "s_waitcnt vmcnt(5)\n\t" \
  "buffer_load_dwordx4 v[188:191], %[vo], %[rs], %[tb] offen offset:3072 sc1\n\t" \
  "s_add_u32 s69, s69, %[aA]\n\t" \
  "s_add_u32 s70, %[rot], 4\n\t" \
  "s_bfe_u32 s70, s70, 0x20001\n\t" \
  "s_waitcnt vmcnt(5)\n\t" \
  "buffer_load_dwordx4 v[20:23], v16, %[rs], %[tb] offen offset:0 sc1\n\t" \
  "s_mul_i32 s70, s70, 4096\n\t" \
  "s_add_u32 s70, s70, %[aA]\n\t" \
  "s_add_u32 s71, %[rot], 6\n\t" \
  "s_waitcnt vmcnt(5)\n\t" \
  "s_add_u32 %[tb], %[rot], 1\n\t" \
  "s_and_b32 %[tb], %[tb], 7\n\t" \
  "s_mul_i32 %[tb], %[tb], 5120\n\t" \
  "s_add_u32 %[tb], %[tb], %[bB]\n\t" \
  "buffer_load_dwordx4 v[24:27], %[vo], %[rs], %[tb] offen offset:0 sc1\n\t" \
  "s_bfe_u32 s71, s71, 0x20001\n\t" \
  "s_mul_i32 s71, s71, 4096\n\t" \
  "s_add_u32 s71, s71, %[aA]\n\t" \
  "s_waitcnt vmcnt(5)\n\t" \
  "buffer_load_dwordx4 v[28:31], %[vo], %[rs], %[tb] offen offset:1024 sc1\n\t" \
  "s_add_u32 s72, %[rot], 7\n\t" \
  "s_and_b32 s72, s72, 7\n\t" \
  "s_mul_i32 s72, s72, 5120\n\t" \
  "s_waitcnt vmcnt(5)\n\t" \
  "buffer_load_dwordx4 v[32:35], %[vo], %[rs], %[tb] offen offset:2048 sc1\n\t" \
  "s_add_u32 s72, s72, %[bB]\n\t" \
  "s_add_u32 s73, %[rot], 0\n\t" \
  "s_bfe_u32 s73, s73, 0x20001\n\t" \
  "s_waitcnt vmcnt(5)\n\t" \
  "buffer_load_dwordx4 v[36:39], %[vo], %[rs], %[tb] offen offset:3072 sc1\n\t" \
  "s_mul_i32 s73, s73, 5120\n\t" \
  "s_add_u32 s73, s73, %[bA]\n\t" \
  "s_add_u32 s74, %[rot], 2\n\t" \
  "s_waitcnt vmcnt(5)\n\t" \
  "buffer_load_dwordx4 v[40:43], v16, %[rs], %[tb] offen offset:0 sc1\n\t" \
  "s_bfe_u32 s74, s74, 0x20001\n\t" \
  "s_mul_i32 s74, s74, 5120\n\t" \
  "s_add_u32 s74, s74, %[bA]\n\t" \
  "s_waitcnt vmcnt(5)\n\t" \
  "s_add_u32 %[tb], %[rot], 2\n\t" \
  "s_and_b32 %[tb], %[tb], 7\n\t" \
  "s_mul_i32 %[tb], %[tb], 5120\n\t" \
  "s_add_u32 %[tb], %[tb], %[bB]\n\t" \
  "buffer_load_dwordx4 v[44:47], %[vo], %[rs], %[tb] offen offset:0 sc1\n\t" \
  "s_add_u32 s76, %[r4], 0\n\t" \
  "s_and_b32 s76, s76, 3\n\t" \
  "s_mul_i32 s76, s76, 20480\n\t" \
  "s_waitcnt vmcnt(5)\n\t" \
  "buffer_load_dwordx4 v[48:51], %[vo], %[rs], %[tb] offen offset:1024 sc1\n\t" \
  "s_add_u32 s76, s76, %[aO]\n\t" \
  "s_add_u32 s77, s76, 4096\n\t" \
  "s_add_u32 s78, s76, 8192\n\t" \
  "s_waitcnt vmcnt(5)\n\t" \
  "buffer_load_dwordx4 v[52:55], %[vo], %[rs], %[tb] offen offset:2048 sc1\n\t" \
  "s_add_u32 s79, s76, 12288\n\t" \
  "s_add_u32 s80, s76, 16384\n\t" \
  "s_add_u32 s81, %[r4], 1\n\t" \
  "s_waitcnt vmcnt(5)\n\t" \
  "buffer_load_dwordx4 v[56:59], %[vo], %[rs], %[tb] offen offset:3072 sc1\n\t" \
  "s_and_b32 s81, s81, 3\n\t" \
  "s_mul_i32 s81, s81, 20480\n\t" \
  "s_add_u32 s81, s81, %[aO]\n\t" \
  "s_waitcnt vmcnt(5)\n\t" \
  "buffer_load_dwordx4 v[60:63], v16, %[rs], %[tb] offen offset:0 sc1\n\t" \
  "s_add_u32 s82, s81, 4096\n\t" \
  "s_add_u32 s83, s81, 8192\n\t" \
  "s_add_u32 s84, s81, 12288\n\t" \
  "s_waitcnt vmcnt(5)\n\t" \
  "s_add_u32 %[tb], %[rot], 3\n\t" \
  "s_and_b32 %[tb], %[tb], 7\n\t" \
  "s_mul_i32 %[tb], %[tb], 5120\n\t" \
  "s_add_u32 %[tb], %[tb], %[bB]\n\t" \
  "buffer_load_dwordx4 v[64:67], %[vo], %[rs], %[tb] offen offset:0 sc1\n\t" \
  "s_add_u32 s85, s81, 16384\n\t" \
  "s_add_u32 s86, %[r4], 2\n\t" \
  "s_and_b32 s86, s86, 3\n\t" \
  "s_waitcnt vmcnt(5)\n\t" \
  "buffer_load_dwordx4 v[68:71], %[vo], %[rs], %[tb] offen offset:1024 sc1\n\t" \
  "s_mul_i32 s86, s86, 20480\n\t" \
  "s_add_u32 s86, s86, %[aO]\n\t" \
  "s_add_u32 s87, s86, 4096\n\t" \
  "s_waitcnt vmcnt(5)\n\t" \
  "buffer_load_dwordx4 v[72:75], %[vo], %[rs], %[tb] offen offset:2048 sc1\n\t" \
  "s_add_u32 s88, s86, 8192\n\t" \
  "s_add_u32 s89, s86, 12288\n\t" \
  "s_add_u32 s90, s86, 16384\n\t" \
  "s_waitcnt vmcnt(5)\n\t" \
  "buffer_load_dwordx4 v[76:79], %[vo], %[rs], %[tb] offen offset:3072 sc1\n\t" \
  "s_add_u32 s91, %[r4], 3\n\t" \
  "s_and_b32 s91, s91, 3\n\t" \
  "s_mul_i32 s91, s91, 20480\n\t" \
  "s_waitcnt vmcnt(5)\n\t" \
  "buffer_load_dwordx4 v[80:83], v16, %[rs], %[tb] offen offset:0 sc1\n\t" \
  "s_add_u32 s91, s91, %[aO]\n\t" \
  "s_add_u32 s92, s91, 4096\n\t" \
  "s_add_u32 s93, s91, 8192\n\t" \
  "s_waitcnt vmcnt(5)\n\t" \
  "s_add_u32 %[tb], %[rot], 4\n\t" \
  "s_and_b32 %[tb], %[tb], 7\n\t" \
  "s_mul_i32 %[tb], %[tb], 5120\n\t" \
  "s_add_u32 %[tb], %[tb], %[bB]\n\t" \
  "buffer_load_dwordx4 v[84:87], %[vo], %[rs], %[tb] offen offset:0 sc1\n\t" \
  "s_add_u32 s94, s91, 12288\n\t" \
  "s_add_u32 s95, s91, 16384\n\t" \
  "s_waitcnt vmcnt(5)\n\t" \
  "buffer_load_dwordx4 v[88:91], %[vo], %[rs], %[tb] offen offset:1024 sc1\n\t" \
  "s_waitcnt vmcnt(5)\n\t" \
  "buffer_load_dwordx4 v[92:95], %[vo], %[rs], %[tb] offen offset:2048 sc1\n\t" \
  "s_waitcnt vmcnt(5)\n\t" \
  "buffer_load_dwordx4 v[96:99], %[vo], %[rs], %[tb] offen offset:3072 sc1\n\t" \
  "s_waitcnt vmcnt(5)\n\t" \
  "buffer_load_dwordx4 v[100:103], v16, %[rs], %[tb] offen offset:0 sc1\n\t" \
  "s_waitcnt vmcnt(5)\n\t" \
  "s_add_u32 %[tb], %[rot], 5\n\t" \
  "s_and_b32 %[tb], %[tb], 7\n\t" \
  "s_mul_i32 %[tb], %[tb], 5120\n\t" \
  "s_add_u32 %[tb], %[tb], %[bB]\n\t" \
  "buffer_load_dwordx4 v[104:107], %[vo], %[rs], %[tb] offen offset:0 sc1\n\t" \
  "s_waitcnt vmcnt(5)\n\t" \
  "buffer_load_dwordx4 v[108:111], %[vo], %[rs], %[tb] offen offset:1024 sc1\n\t" \
  "s_waitcnt vmcnt(5)\n\t" \
  "buffer_load_dwordx4 v[112:115], %[vo], %[rs], %[tb] offen offset:2048 sc1\n\t" \
  "s_waitcnt vmcnt(5)\n\t" \
  "buffer_load_dwordx4 v[116:119], %[vo], %[rs], %[tb] offen offset:3072 sc1\n\t" \
  "s_waitcnt vmcnt(5)\n\t" \
  "buffer_load_dwordx4 v[120:123], v16, %[rs], %[tb] offen offset:0 sc1\n\t" \
  "s_waitcnt vmcnt(5)\n\t" \
  "s_mul_i32 %[cnt], %[w], 3\n\t" \
  "s_add_u32 %[cnt], %[cnt], 9\n\t" \
  "s_add_u32 %[tb], %[cnt], %[r20]\n\t" \
  "s_sub_u32 %[ta], %[tb], 20\n\t" \
  "s_cselect_b32 %[tb], %[tb], %[ta]\n\t" \
  "s_mul_i32 %[tb], %[tb], 5120\n\t" \
  "s_add_u32 %[tb], %[tb], %[bO]\n\t" \
  "s_mul_i32 %[cnt], %[cnt], 5120\n\t" \
  "s_add_u32 %[cnt], %[cnt], 45056\n\t" \
  "s_add_u32 m0, %[cnt], %[l0]\n\t" \
  "s_nop 0\n\t" \
  "buffer_load_dwordx4 %[vo], %[rs], %[tb] offen offset:0 sc1 lds\n\t" \
  "s_waitcnt vmcnt(5)\n\t" \
  "buffer_load_dwordx4 %[vo], %[rs], %[tb] offen offset:1024 sc1 lds\n\t" \
  "s_waitcnt vmcnt(5)\n\t" \
  "buffer_load_dwordx4 %[vo], %[rs], %[tb] offen offset:2048 sc1 lds\n\t" \
  "s_waitcnt vmcnt(5)\n\t" \
  "buffer_load_dwordx4 %[vo], %[rs], %[tb] offen offset:3072 sc1 lds\n\t" \
  "s_waitcnt vmcnt(5)\n\t" \
  "s_add_u32 m0, m0, 4096\n\t" \
  "s_nop 0\n\t" \
  "buffer_load_dwordx4 v16, %[rs], %[tb] offen offset:0 sc1 lds\n\t" \
  "s_waitcnt vmcnt(5)\n\t" \
  "s_mul_i32 %[cnt], %[w], 3\n\t" \
  "s_add_u32 %[cnt], %[cnt], 10\n\t" \
  "s_add_u32 %[tb], %[cnt], %[r20]\n\t" \
  "s_sub_u32 %[ta], %[tb], 20\n\t" \
  "s_cselect_b32 %[tb], %[tb], %[ta]\n\t" \
  "s_mul_i32 %[tb], %[tb], 5120\n\t" \
  "s_add_u32 %[tb], %[tb], %[bO]\n\t" \
  "s_mul_i32 %[cnt], %[cnt], 5120\n\t" \
  "s_add_u32 %[cnt], %[cnt], 45056\n\t" \
  "s_add_u32 m0, %[cnt], %[l0]\n\t" \
  "s_nop 0\n\t" \
  "buffer_load_dwordx4 %[vo], %[rs], %[tb] offen offset:0 sc1 lds\n\t" \
  "s_waitcnt vmcnt(5)\n\t" \
  "buffer_load_dwordx4 %[vo], %[rs], %[tb] offen offset:1024 sc1 lds\n\t" \
  "s_waitcnt vmcnt(5)\n\t" \
  "buffer_load_dwordx4 %[vo], %[rs], %[tb] offen offset:2048 sc1 lds\n\t" \
  "s_waitcnt vmcnt(5)\n\t" \
  "buffer_load_dwordx4 %[vo], %[rs], %[tb] offen offset:3072 sc1 lds\n\t" \
  "s_waitcnt vmcnt(5)\n\t" \
  "s_add_u32 m0, m0, 4096\n\t" \
  "s_nop 0\n\t" \
  "buffer_load_dwordx4 v16, %[rs], %[tb] offen offset:0 sc1 lds\n\t" \
  "s_waitcnt vmcnt(5)\n\t" \
  "s_add_u32 %[tb], %[rot], 4\n\t" \
  "s_bfe_u32 %[tb], %[tb], 0x20001\n\t" \
  "s_mul_i32 %[tb], %[tb], 5120\n\t" \
  "s_add_u32 %[tb], %[tb], %[bA]\n\t" \
  "s_mul_i32 %[cnt], %[w], 10240\n\t" \
  "s_add_u32 %[cnt], %[cnt], 0\n\t" \
  "s_add_u32 m0, %[cnt], %[l0]\n\t" \
  "s_nop 0\n\t" \
  "buffer_load_dwordx4 %[vo], %[rs], %[tb] offen offset:0 sc1 lds\n\t" \
  "s_waitcnt vmcnt(5)\n\t" \
  "buffer_load_dwordx4 %[vo], %[rs], %[tb] offen offset:1024 sc1 lds\n\t" \
  "s_waitcnt vmcnt(5)\n\t" \
  "buffer_load_dwordx4 %[vo], %[rs], %[tb] offen offset:2048 sc1 lds\n\t" \
  "s_waitcnt vmcnt(5)\n\t" \
  "buffer_load_dwordx4 %[vo], %[rs], %[tb] offen offset:3072 sc1 lds\n\t" \
  "s_waitcnt vmcnt(5)\n\t" \
  "s_add_u32 m0, m0, 4096\n\t" \
  "s_nop 0\n\t" \
  "buffer_load_dwordx4 v16, %[rs], %[tb] offen offset:0 sc1 lds\n\t" \
  "s_waitcnt vmcnt(5)\n\t" \
  "s_add_u32 %[tb], %[rot], 6\n\t" \
  "s_bfe_u32 %[tb], %[tb], 0x20001\n\t" \
  "s_mul_i32 %[tb], %[tb], 5120\n\t" \
  "s_add_u32 %[tb], %[tb], %[bA]\n\t" \
  "s_mul_i32 %[cnt], %[w], 10240\n\t" \
  "s_add_u32 %[cnt], %[cnt], 5120\n\t" \
  "s_add_u32 m0, %[cnt], %[l0]\n\t" \
  "s_nop 0\n\t" \
  "buffer_load_dwordx4 %[vo], %[rs], %[tb] offen offset:0 sc1 lds\n\t" \
  "s_waitcnt vmcnt(5)\n\t" \
  "buffer_load_dwordx4 %[vo], %[rs], %[tb] offen offset:1024 sc1 lds\n\t" \
  "s_waitcnt vmcnt(5)\n\t" \
  "buffer_load_dwordx4 %[vo], %[rs], %[tb] offen offset:2048 sc1 lds\n\t" \
  "s_waitcnt vmcnt(5)\n\t" \
  "buffer_load_dwordx4 %[vo], %[rs], %[tb] offen offset:3072 sc1 lds\n\t" \
  "s_waitcnt vmcnt(5)\n\t" \
  "s_add_u32 m0, m0, 4096\n\t" \
  "s_nop 0\n\t" \
  "buffer_load_dwordx4 v16, %[rs], %[tb] offen offset:0 sc1 lds\n\t" \
  "s_waitcnt vmcnt(5)\n\t" \
  "buffer_load_dwordx2 v[8:9], v11, %[rs], %[f1B] offen sc1\n\t" \
  "s_waitcnt vmcnt(5)\n\t" \
  "s_add_u32 %[tb], %[rot], 6\n\t" \
  "s_and_b32 %[tb], %[tb], 7\n\t" \
  "s_mul_i32 %[tb], %[tb], 5120\n\t" \
  "s_add_u32 %[tb], %[tb], %[bB]\n\t" \
  "buffer_load_dwordx4 v[124:127], %[vo], %[rs], %[tb] offen offset:0 sc1\n\t" \
  "s_waitcnt vmcnt(5)\n\t" \
  "buffer_load_dwordx4 v[128:131], %[vo], %[rs], %[tb] offen offset:1024 sc1\n\t" \
  "s_waitcnt vmcnt(5)\n\t" \
  "buffer_load_dwordx4 v[132:135], %[vo], %[rs], %[tb] offen offset:2048 sc1\n\t" \
  "s_waitcnt vmcnt(5)\n\t" \
  "buffer_load_dwordx4 v[136:139], %[vo], %[rs], %[tb] offen offset:3072 sc1\n\t" \
  "s_waitcnt vmcnt(5)\n\t" \
  "buffer_load_dwordx4 v[140:143], v16, %[rs], %[tb] offen offset:0 sc1\n\t" \
  "s_waitcnt vmcnt(5)\n\t" \
  "v_cmp_eq_u64 vcc, %[tag], v[8:9]\n\t" \
  "s_cmp_eq_u64 vcc, exec\n\t" \
  "s_cbranch_scc1 P2K0_%=\n\t" \
  "s_mov_b32 %[cnt], 0x10000\n\t" \
  "P2L0_%=:\n\t" \
  "s_sleep 1\n\t" \
  "buffer_load_dwordx2 v[8:9], v11, %[rs], %[f1B] offen sc1\n\t" \
  "s_sub_u32 %[cnt], %[cnt], 1\n\t" \
  "s_waitcnt vmcnt(0)\n\t" \
  "v_cmp_eq_u64 vcc, %[tag], v[8:9]\n\t" \
  "s_cmp_eq_u64 vcc, exec\n\t" \
  "s_cbranch_scc1 P2K0_%=\n\t" \
  "s_cmp_lg_u32 %[cnt], 0\n\t" \
  "s_cbranch_scc1 P2L0_%=\n\t" \
  "P2K0_%=:\n\t" \
  "buffer_load_dwordx4 v[192:195], v12, %[rs], s60 offen sc1\n\t" \
  "buffer_load_dwordx4 v[212:215], v13, %[rs], s60 offen sc1\n\t" \
  "buffer_load_dwordx4 v[232:235], v14, %[rs], s60 offen sc1\n\t" \
  "buffer_load_dwordx4 v[252:255], v15, %[rs], s60 offen sc1\n\t" \
  "buffer_load_dwordx4 v[144:147], v12, %[rs], s61 offen sc1\n\t" \
  "buffer_load_dwordx4 v[148:151], v13, %[rs], s61 offen sc1\n\t" \
  "buffer_load_dwordx4 v[152:155], v14, %[rs], s61 offen sc1\n\t" \
  "s_mov_b64 exec, 1\n\t" \
  "ds_add_u32 v18, v19 offset:32\n\t" \
  "s_mov_b64 exec, -1\n\t" \
  "v_add_u32 v11, 31744, v11\n\t" \
  "s_waitcnt vmcnt(3)\n\t" \
  "v_mfma_f32_16x16x32_bf16 v[216:219], v[176:179], v[232:235], 0\n\t" \
  "buffer_load_dwordx4 v[156:159], v15, %[rs], s61 offen sc1\n\t" \
  "v_mfma_f32_16x16x32_bf16 v[236:239], v[176:179], v[252:255], 0\n\t" \
  "buffer_load_dwordx4 v[160:163], v12, %[rs], s62 offen sc1\n\t" \
  "v_mfma_f32_16x16x32_bf16 v[196:199], v[176:179], v[212:215], 0\n\t" \
  "buffer_load_dwordx4 v[164:167], v13, %[rs], s62 offen sc1\n\t" \
  "v_mfma_f32_16x16x32_bf16 v[176:179], v[176:179], v[192:195], 0\n\t" \
  "buffer_load_dwordx4 v[168:171], v14, %[rs], s62 offen sc1\n\t" \
  "v_mfma_f32_16x16x32_bf16 v[220:223], v[180:183], v[232:235], 0\n\t" \
  "buffer_load_dwordx4 v[172:175], v15, %[rs], s62 offen sc1\n\t" \
  "v_mfma_f32_16x16x32_bf16 v[240:243], v[180:183], v[252:255], 0\n\t" \
  "v_mfma_f32_16x16x32_bf16 v[200:203], v[180:183], v[212:215], 0\n\t" \
  "v_mfma_f32_16x16x32_bf16 v[180:183], v[180:183], v[192:195], 0\n\t" \
  "v_mfma_f32_16x16x32_bf16 v[224:227], v[184:187], v[232:235], 0\n\t" \
  "v_mfma_f32_16x16x32_bf16 v[244:247], v[184:187], v[252:255], 0\n\t" \
  "v_mfma_f32_16x16x32_bf16 v[204:207], v[184:187], v[212:215], 0\n\t" \
  "v_mfma_f32_16x16x32_bf16 v[184:187], v[184:187], v[192:195], 0\n\t" \
  "v_mfma_f32_16x16x32_bf16 v[228:231], v[188:191], v[232:235], 0\n\t" \
  "v_mfma_f32_16x16x32_bf16 v[248:251], v[188:191], v[252:255], 0\n\t" \
  "v_mfma_f32_16x16x32_bf16 v[208:211], v[188:191], v[212:215], 0\n\t" \
  "v_mfma_f32_16x16x32_bf16 v[188:191], v[188:191], v[192:195], 0\n\t" \
  "v_mfma_f32_16x16x32_bf16 v[232:235], v[20:23], v[232:235], 0\n\t" \
  "v_mfma_f32_16x16x32_bf16 v[252:255], v[20:23], v[252:255], 0\n\t" \
  "v_mfma_f32_16x16x32_bf16 v[212:215], v[20:23], v[212:215], 0\n\t" \
  "v_mfma_f32_16x16x32_bf16 v[192:195], v[20:23], v[192:195], 0\n\t" \
  "buffer_load_dwordx4 v[20:23], v12, %[rs], s63 offen sc1\n\t" \
  "s_waitcnt vmcnt(5)\n\t" \
  "v_mfma_f32_16x16x32_bf16 v[216:219], v[24:27], v[152:155], v[216:219]\n\t" \
  "v_mfma_f32_16x16x32_bf16 v[236:239], v[24:27], v[156:159], v[236:239]\n\t" \
  "v_mfma_f32_16x16x32_bf16 v[196:199], v[24:27], v[148:151], v[196:199]\n\t" \
  "v_mfma_f32_16x16x32_bf16 v[176:179], v[24:27], v[144:147], v[176:179]\n\t" \
  "buffer_load_dwordx4 v[24:27], v13, %[rs], s63 offen sc1\n\t" \
  "v_mfma_f32_16x16x32_bf16 v[220:223], v[28:31], v[152:155], v[220:223]\n\t" \
  "v_mfma_f32_16x16x32_bf16 v[240:243], v[28:31], v[156:159], v[240:243]\n\t" \
  "v_mfma_f32_16x16x32_bf16 v[200:203], v[28:31], v[148:151], v[200:203]\n\t" \
  "v_mfma_f32_16x16x32_bf16 v[180:183], v[28:31], v[144:147], v[180:183]\n\t" \
  "buffer_load_dwordx4 v[28:31], v14, %[rs], s63 offen sc1\n\t" \
  "v_mfma_f32_16x16x32_bf16 v[224:227], v[32:35], v[152:155], v[224:227]\n\t" \
  "v_mfma_f32_16x16x32_bf16 v[244:247], v[32:35], v[156:159], v[244:247]\n\t" \
  "v_mfma_f32_16x16x32_bf16 v[204:207], v[32:35], v[148:151], v[204:207]\n\t" \
  "v_mfma_f32_16x16x32_bf16 v[184:187], v[32:35], v[144:147], v[184:187]\n\t" \
  "buffer_load_dwordx4 v[32:35], v15, %[rs], s63 offen sc1\n\t" \
  "v_mfma_f32_16x16x32_bf16 v[228:231], v[36:39], v[152:155], v[228:231]\n\t" \
  "v_mfma_f32_16x16x32_bf16 v[248:251], v[36:39], v[156:159], v[248:251]\n\t" \
  "v_mfma_f32_16x16x32_bf16 v[208:211], v[36:39], v[148:151], v[208:211]\n\t" \
  "v_mfma_f32_16x16x32_bf16 v[188:191], v[36:39], v[144:147], v[188:191]\n\t" \
  "buffer_load_dwordx4 v[36:39], v12, %[rs], s64 offen sc1\n\t" \
  "v_mfma_f32_16x16x32_bf16 v[232:235], v[40:43], v[152:155], v[232:235]\n\t" \
  "buffer_load_dwordx4 v[152:155], v13, %[rs], s64 offen sc1\n\t" \
  "v_mfma_f32_16x16x32_bf16 v[252:255], v[40:43], v[156:159], v[252:255]\n\t" \
  "buffer_load_dwordx4 v[156:159], v14, %[rs], s64 offen sc1\n\t" \
  "v_mfma_f32_16x16x32_bf16 v[212:215], v[40:43], v[148:151], v[212:215]\n\t" \
  "buffer_load_dwordx4 v[148:151], v15, %[rs], s64 offen sc1\n\t" \
  "v_mfma_f32_16x16x32_bf16 v[192:195], v[40:43], v[144:147], v[192:195]\n\t" \
  "buffer_load_dwordx2 v[8:9], v11, %[rs], %[f1B] offen sc1\n\t" \
  "s_waitcnt vmcnt(9)\n\t" \
  "v_mfma_f32_16x16x32_bf16 v[216:219], v[44:47], v[168:171], v[216:219]\n\t" \
  "buffer_load_dwordx4 v[40:43], v12, %[rs], s65 offen sc1\n\t" \
  "v_mfma_f32_16x16x32_bf16 v[236:239], v[44:47], v[172:175], v[236:239]\n\t" \
  "buffer_load_dwordx4 v[144:147], v13, %[rs], s65 offen sc1\n\t" \
  "v_mfma_f32_16x16x32_bf16 v[196:199], v[44:47], v[164:167], v[196:199]\n\t" \
  "v_mfma_f32_16x16x32_bf16 v[176:179], v[44:47], v[160:163], v[176:179]\n\t" \
  "buffer_load_dwordx4 v[44:47], v14, %[rs], s65 offen sc1\n\t" \
  "v_mfma_f32_16x16x32_bf16 v[220:223], v[48:51], v[168:171], v[220:223]\n\t" \
  "v_mfma_f32_16x16x32_bf16 v[240:243], v[48:51], v[172:175], v[240:243]\n\t" \
  "v_mfma_f32_16x16x32_bf16 v[200:203], v[48:51], v[164:167], v[200:203]\n\t" \
  "v_mfma_f32_16x16x32_bf16 v[180:183], v[48:51], v[160:163], v[180:183]\n\t" \
  "v_mfma_f32_16x16x32_bf16 v[224:227], v[52:55], v[168:171], v[224:227]\n\t" \
  "v_mfma_f32_16x16x32_bf16 v[244:247], v[52:55], v[172:175], v[244:247]\n\t" \
  "v_mfma_f32_16x16x32_bf16 v[204:207], v[52:55], v[164:167], v[204:207]\n\t" \
  "v_mfma_f32_16x16x32_bf16 v[184:187], v[52:55], v[160:163], v[184:187]\n\t" \
  "v_mfma_f32_16x16x32_bf16 v[228:231], v[56:59], v[168:171], v[228:231]\n\t" \
  "v_mfma_f32_16x16x32_bf16 v[248:251], v[56:59], v[172:175], v[248:251]\n\t" \
  "v_mfma_f32_16x16x32_bf16 v[208:211], v[56:59], v[164:167], v[208:211]\n\t" \
  "v_mfma_f32_16x16x32_bf16 v[188:191], v[56:59], v[160:163], v[188:191]\n\t" \
  "v_mfma_f32_16x16x32_bf16 v[232:235], v[60:63], v[168:171], v[232:235]\n\t" \
  "v_mfma_f32_16x16x32_bf16 v[252:255], v[60:63], v[172:175], v[252:255]\n\t" \
  "v_mfma_f32_16x16x32_bf16 v[212:215], v[60:63], v[164:167], v[212:215]\n\t" \
  "v_mfma_f32_16x16x32_bf16 v[192:195], v[60:63], v[160:163], v[192:195]\n\t" \
  "s_waitcnt vmcnt(8)\n\t" \
  "v_mfma_f32_16x16x32_bf16 v[216:219], v[64:67], v[28:31], v[216:219]\n\t" \
  "buffer_load_dwordx4 v[172:175], v15, %[rs], s65 offen sc1\n\t" \
  "v_mfma_f32_16x16x32_bf16 v[236:239], v[64:67], v[32:35], v[236:239]\n\t" \
  "buffer_load_dwordx4 v[164:167], v12, %[rs], s66 offen sc1\n\t" \
  "v_mfma_f32_16x16x32_bf16 v[196:199], v[64:67], v[24:27], v[196:199]\n\t" \
  "buffer_load_dwordx4 v[60:63], v13, %[rs], s66 offen sc1\n\t" \
  "v_mfma_f32_16x16x32_bf16 v[176:179], v[64:67], v[20:23], v[176:179]\n\t" \
  "buffer_load_dwordx4 v[160:163], v14, %[rs], s66 offen sc1\n\t" \
  "v_mfma_f32_16x16x32_bf16 v[220:223], v[68:71], v[28:31], v[220:223]\n\t" \
  "s_add_u32 %[ta], %[l0], 45056\n\t" \
  "v_add_u32 v48, %[ta], %[vo]\n\t" \
  "v_mfma_f32_16x16x32_bf16 v[240:243], v[68:71], v[32:35], v[240:243]\n\t" \
  "v_add_u32 v49, 0xf000, v48\n\t" \
  "v_mfma_f32_16x16x32_bf16 v[200:203], v[68:71], v[24:27], v[200:203]\n\t" \
  "s_xor_b32 %[ta], %[w], 2\n\t" \
  "s_mul_i32 %[ta], %[ta], 10240\n\t" \
  "v_mfma_f32_16x16x32_bf16 v[180:183], v[68:71], v[20:23], v[180:183]\n\t" \
  "s_add_u32 %[ta], %[ta], %[l0]\n\t" \
  "v_add_u32 v50, %[ta], %[vo]\n\t" \
  "v_mfma_f32_16x16x32_bf16 v[224:227], v[72:75], v[28:31], v[224:227]\n\t" \
  "s_xor_b32 %[ta], %[w], 3\n\t" \
  "s_mul_i32 %[ta], %[ta], 10240\n\t" \
  "v_mfma_f32_16x16x32_bf16 v[244:247], v[72:75], v[32:35], v[244:247]\n\t" \
  "s_add_u32 %[ta], %[ta], %[l0]\n\t" \
  "v_add_u32 v51, %[ta], %[vo]\n\t" \
  "v_mfma_f32_16x16x32_bf16 v[204:207], v[72:75], v[24:27], v[204:207]\n\t" \
  "s_and_b32 %[ta], %[w], 1\n\t" \
  "v_lshl_add_u32 v52, %[ta], 2, v18\n\t" \
  "v_mfma_f32_16x16x32_bf16 v[184:187], v[72:75], v[20:23], v[184:187]\n\t" \
  "s_lshr_b32 %[ta], %[w], 1\n\t" \
  "v_lshl_add_u32 v53, %[ta], 2, v18\n\t" \
  "v_mfma_f32_16x16x32_bf16 v[228:231], v[76:79], v[28:31], v[228:231]\n\t" \
  "v_bfe_u32 v169, %[vo], 4, 4\n\t" \
  "v_mfma_f32_16x16x32_bf16 v[248:251], v[76:79], v[32:35], v[248:251]\n\t" \
  "v_mul_u32_u24 v169, 160, v169\n\t" \
  "v_mfma_f32_16x16x32_bf16 v[208:211], v[76:79], v[24:27], v[208:211]\n\t" \
  "v_lshrrev_b32 v54, 8, %[vo]\n\t" \
  "v_mfma_f32_16x16x32_bf16 v[188:191], v[76:79], v[20:23], v[188:191]\n\t" \
  "v_mul_u32_u24 v54, 248, v54\n\t" \
  "v_mfma_f32_16x16x32_bf16 v[232:235], v[80:83], v[28:31], v[232:235]\n\t" \
  "v_sub_u32 v169, v169, v54\n\t" \
  "v_mfma_f32_16x16x32_bf16 v[252:255], v[80:83], v[32:35], v[252:255]\n\t" \
  "v_add_u32 v54, v169, v50\n\t" \
  "v_mfma_f32_16x16x32_bf16 v[212:215], v[80:83], v[24:27], v[212:215]\n\t" \
  "v_add_u32 v58, 0, %[vo]\n\t" \
  "v_mfma_f32_16x16x32_bf16 v[192:195], v[80:83], v[20:23], v[192:195]\n\t" \
  "v_mul_u32_u24 v169, 0x19a, v58\n\t" \
  "s_waitcnt vmcnt(8)\n\t" \
  "v_mfma_f32_16x16x32_bf16 v[216:219], v[84:87], v[156:159], v[216:219]\n\t" \
  "buffer_load_dwordx4 v[64:67], v15, %[rs], s66 offen sc1\n\t" \
  "v_mfma_f32_16x16x32_bf16 v[236:239], v[84:87], v[148:151], v[236:239]\n\t" \
  "buffer_load_dwordx4 v[68:71], %[vo], %[rs], s72 offen offset:0 sc1\n\t" \
  "v_mfma_f32_16x16x32_bf16 v[196:199], v[84:87], v[152:155], v[196:199]\n\t" \
  "buffer_load_dwordx4 v[72:75], %[vo], %[rs], s72 offen offset:1024 sc1\n\t" \
  "v_mfma_f32_16x16x32_bf16 v[176:179], v[84:87], v[36:39], v[176:179]\n\t" \
  "buffer_load_dwordx4 v[76:79], %[vo], %[rs], s72 offen offset:2048 sc1\n\t" \
  "v_mfma_f32_16x16x32_bf16 v[220:223], v[88:91], v[156:159], v[220:223]\n\t" \
  "v_lshrrev_b32 v169, 16, v169\n\t" \
  "v_mfma_f32_16x16x32_bf16 v[240:243], v[88:91], v[148:151], v[240:243]\n\t" \
  "v_lshl_add_u32 v55, v169, 4, v50\n\t" \
  "v_mfma_f32_16x16x32_bf16 v[200:203], v[88:91], v[152:155], v[200:203]\n\t" \
  "v_mul_u32_u24 v169, 1120, v169\n\t" \
  "v_mfma_f32_16x16x32_bf16 v[180:183], v[88:91], v[36:39], v[180:183]\n\t" \
  "v_add_u32 v58, v169, v58\n\t" \
  "v_mfma_f32_16x16x32_bf16 v[224:227], v[92:95], v[156:159], v[224:227]\n\t" \
  "v_add_u32 v59, 1024, %[vo]\n\t" \
  "v_mfma_f32_16x16x32_bf16 v[244:247], v[92:95], v[148:151], v[244:247]\n\t" \
  "v_mul_u32_u24 v169, 0x19a, v59\n\t" \
  "v_mfma_f32_16x16x32_bf16 v[204:207], v[92:95], v[152:155], v[204:207]\n\t" \
  "v_lshrrev_b32 v169, 16, v169\n\t" \
  "v_mfma_f32_16x16x32_bf16 v[184:187], v[92:95], v[36:39], v[184:187]\n\t" \
  "v_lshl_add_u32 v56, v169, 4, v50\n\t" \
  "v_mfma_f32_16x16x32_bf16 v[228:231], v[96:99], v[156:159], v[228:231]\n\t" \
  "v_mul_u32_u24 v169, 1120, v169\n\t" \
  "v_mfma_f32_16x16x32_bf16 v[248:251], v[96:99], v[148:151], v[248:251]\n\t" \
  "v_add_u32 v59, v169, v59\n\t" \
  "v_mfma_f32_16x16x32_bf16 v[208:211], v[96:99], v[152:155], v[208:211]\n\t" \
  "v_add_u32 v168, 2048, %[vo]\n\t" \
  "v_mfma_f32_16x16x32_bf16 v[188:191], v[96:99], v[36:39], v[188:191]\n\t" \
  "v_mul_u32_u24 v169, 0x19a, v168\n\t" \
  "v_mfma_f32_16x16x32_bf16 v[232:235], v[100:103], v[156:159], v[232:235]\n\t" \
  "v_lshrrev_b32 v169, 16, v169\n\t" \
  "v_mfma_f32_16x16x32_bf16 v[252:255], v[100:103], v[148:151], v[252:255]\n\t" \
  "v_lshl_add_u32 v57, v169, 4, v50\n\t" \
  "v_mfma_f32_16x16x32_bf16 v[212:215], v[100:103], v[152:155], v[212:215]\n\t" \
  "v_mul_u32_u24 v169, 1120, v169\n\t" \
  "v_mfma_f32_16x16x32_bf16 v[192:195], v[100:103], v[36:39], v[192:195]\n\t" \
  "v_add_u32 v168, v169, v168\n\t" \
  "s_waitcnt vmcnt(7)\n\t" \
  "v_mfma_f32_16x16x32_bf16 v[216:219], v[104:107], v[44:47], v[216:219]\n\t" \
  "buffer_load_dwordx4 v[28:31], %[vo], %[rs], s72 offen offset:3072 sc1\n\t" \
  "v_mfma_f32_16x16x32_bf16 v[236:239], v[104:107], v[172:175], v[236:239]\n\t" \
  "buffer_load_dwordx4 v[32:35], v16, %[rs], s72 offen offset:0 sc1\n\t" \
  "v_mfma_f32_16x16x32_bf16 v[196:199], v[104:107], v[144:147], v[196:199]\n\t" \
  "buffer_load_dwordx4 v[24:27], v12, %[rs], s67 offen sc1\n\t" \
  "v_mfma_f32_16x16x32_bf16 v[176:179], v[104:107], v[40:43], v[176:179]\n\t" \
  "buffer_load_dwordx4 v[80:83], v13, %[rs], s67 offen sc1\n\t" \
  "v_mfma_f32_16x16x32_bf16 v[220:223], v[108:111], v[44:47], v[220:223]\n\t" \
  "buffer_load_dwordx4 v[20:23], v14, %[rs], s67 offen sc1\n\t" \
  "v_mfma_f32_16x16x32_bf16 v[240:243], v[108:111], v[172:175], v[240:243]\n\t" \
  "v_lshrrev_b32 v169, 9, %[vo]\n\t" \
  "v_mfma_f32_16x16x32_bf16 v[200:203], v[108:111], v[144:147], v[200:203]\n\t" \
  "v_lshl_add_u32 v168, v169, 30, v168\n\t" \
  "v_mfma_f32_16x16x32_bf16 v[180:183], v[108:111], v[40:43], v[180:183]\n\t" \
  "ds_read_b32 v170, v18 offset:32\n\t" \
  "v_mfma_f32_16x16x32_bf16 v[224:227], v[112:115], v[44:47], v[224:227]\n\t" \
  "v_mfma_f32_16x16x32_bf16 v[244:247], v[112:115], v[172:175], v[244:247]\n\t" \
  "v_mfma_f32_16x16x32_bf16 v[204:207], v[112:115], v[144:147], v[204:207]\n\t" \
  "v_mfma_f32_16x16x32_bf16 v[184:187], v[112:115], v[40:43], v[184:187]\n\t" \
  "s_waitcnt lgkmcnt(0)\n\t" \
  "v_cmp_le_u32 vcc, 4, v170\n\t" \
  "s_cmp_eq_u64 vcc, exec\n\t" \
  "s_cbranch_scc1 P2K1_%=\n\t" \
  "s_mov_b32 %[cnt], 0x400000\n\t" \
  "P2L1_%=:\n\t" \
  "ds_read_b32 v170, v18 offset:32\n\t" \
  "s_sub_u32 %[cnt], %[cnt], 1\n\t" \
  "s_waitcnt lgkmcnt(0)\n\t" \
  "v_cmp_le_u32 vcc, 4, v170\n\t" \
  "s_cmp_eq_u64 vcc, exec\n\t" \
  "s_cbranch_scc1 P2K1_%=\n\t" \
  "s_cmp_lg_u32 %[cnt], 0\n\t" \
  "s_cbranch_scc1 P2L1_%=\n\t" \
  "P2K1_%=:\n\t" \
  "v_mfma_f32_16x16x32_bf16 v[228:231], v[116:119], v[44:47], v[228:231]\n\t" \
  "v_mfma_f32_16x16x32_bf16 v[248:251], v[116:119], v[172:175], v[248:251]\n\t" \
  "v_mfma_f32_16x16x32_bf16 v[208:211], v[116:119], v[144:147], v[208:211]\n\t" \
  "v_mfma_f32_16x16x32_bf16 v[188:191], v[116:119], v[40:43], v[188:191]\n\t" \
  "v_mfma_f32_16x16x32_bf16 v[232:235], v[120:123], v[44:47], v[232:235]\n\t" \
  "v_mfma_f32_16x16x32_bf16 v[252:255], v[120:123], v[172:175], v[252:255]\n\t" \
  "v_mfma_f32_16x16x32_bf16 v[212:215], v[120:123], v[144:147], v[212:215]\n\t" \
  "v_mfma_f32_16x16x32_bf16 v[192:195], v[120:123], v[40:43], v[192:195]\n\t" \
  "s_waitcnt vmcnt(8)\n\t" \
  "v_mfma_f32_16x16x32_bf16 v[216:219], v[124:127], v[160:163], v[216:219]\n\t" \
  "buffer_load_dwordx4 v[84:87], v15, %[rs], s67 offen sc1\n\t" \
  "v_mfma_f32_16x16x32_bf16 v[236:239], v[124:127], v[64:67], v[236:239]\n\t" \
  "buffer_load_dwordx4 v[88:91], %[vo], %[rs], s73 offen offset:0 sc1\n\t" \
  "v_mfma_f32_16x16x32_bf16 v[196:199], v[124:127], v[60:63], v[196:199]\n\t" \
  "buffer_load_dwordx4 v[92:95], %[vo], %[rs], s73 offen offset:1024 sc1\n\t" \
  "v_mfma_f32_16x16x32_bf16 v[176:179], v[124:127], v[164:167], v[176:179]\n\t" \
  "buffer_load_dwordx4 v[96:99], %[vo], %[rs], s73 offen offset:2048 sc1\n\t" \
  "v_mfma_f32_16x16x32_bf16 v[220:223], v[128:131], v[160:163], v[220:223]\n\t" \
  "v_mfma_f32_16x16x32_bf16 v[240:243], v[128:131], v[64:67], v[240:243]\n\t" \
  "v_mfma_f32_16x16x32_bf16 v[200:203], v[128:131], v[60:63], v[200:203]\n\t" \
  "v_mfma_f32_16x16x32_bf16 v[180:183], v[128:131], v[164:167], v[180:183]\n\t" \
  "v_mfma_f32_16x16x32_bf16 v[224:227], v[132:135], v[160:163], v[224:227]\n\t" \
  "v_mfma_f32_16x16x32_bf16 v[244:247], v[132:135], v[64:67], v[244:247]\n\t" \
  "v_mfma_f32_16x16x32_bf16 v[204:207], v[132:135], v[60:63], v[204:207]\n\t" \
  "v_mfma_f32_16x16x32_bf16 v[184:187], v[132:135], v[164:167], v[184:187]\n\t" \
  "v_mfma_f32_16x16x32_bf16 v[228:231], v[136:139], v[160:163], v[228:231]\n\t" \
  "v_mfma_f32_16x16x32_bf16 v[248:251], v[136:139], v[64:67], v[248:251]\n\t" \
  "v_mfma_f32_16x16x32_bf16 v[208:211], v[136:139], v[60:63], v[208:211]\n\t" \
  "v_mfma_f32_16x16x32_bf16 v[188:191], v[136:139], v[164:167], v[188:191]\n\t" \
  "v_mfma_f32_16x16x32_bf16 v[232:235], v[140:143], v[160:163], v[232:235]\n\t" \
  "v_mfma_f32_16x16x32_bf16 v[252:255], v[140:143], v[64:67], v[252:255]\n\t" \
  "v_mfma_f32_16x16x32_bf16 v[212:215], v[140:143], v[60:63], v[212:215]\n\t" \
  "v_mfma_f32_16x16x32_bf16 v[192:195], v[140:143], v[164:167], v[192:195]\n\t" \
  "s_waitcnt vmcnt(3)\n\t" \
  "v_mfma_f32_16x16x32_bf16 v[216:219], v[68:71], v[20:23], v[216:219]\n\t" \
  "buffer_load_dwordx4 v[156:159], %[vo], %[rs], s73 offen offset:3072 sc1\n\t" \
  "v_mfma_f32_16x16x32_bf16 v[236:239], v[68:71], v[84:87], v[236:239]\n\t" \
  "buffer_load_dwordx4 v[148:151], v16, %[rs], s73 offen offset:0 sc1\n\t" \
  "v_mfma_f32_16x16x32_bf16 v[196:199], v[68:71], v[80:83], v[196:199]\n\t" \
  "v_cmp_eq_u64 vcc, %[tag], v[8:9]\n\t" \
  "s_cmp_eq_u64 vcc, exec\n\t" \
  "s_cbranch_scc1 P2K2_%=\n\t" \
  "s_mov_b32 %[cnt], 0x10000\n\t" \
  "P2L2_%=:\n\t" \
  "s_sleep 1\n\t" \
  "buffer_load_dwordx2 v[8:9], v11, %[rs], %[f1B] offen sc1\n\t" \
  "s_sub_u32 %[cnt], %[cnt], 1\n\t" \
  "s_waitcnt vmcnt(0)\n\t" \
  "v_cmp_eq_u64 vcc, %[tag], v[8:9]\n\t" \
  "s_cmp_eq_u64 vcc, exec\n\t" \
  "s_cbranch_scc1 P2K2_%=\n\t" \
  "s_cmp_lg_u32 %[cnt], 0\n\t" \
  "s_cbranch_scc1 P2L2_%=\n\t" \
  "P2K2_%=:\n\t" \
  "v_mfma_f32_16x16x32_bf16 v[176:179], v[68:71], v[24:27], v[176:179]\n\t" \
  "buffer_load_dwordx4 v[152:155], v12, %[rs], s68 offen sc1\n\t" \
  "v_mfma_f32_16x16x32_bf16 v[220:223], v[72:75], v[20:23], v[220:223]\n\t" \
  "buffer_load_dwordx4 v[100:103], v13, %[rs], s68 offen sc1\n\t" \
  "v_mfma_f32_16x16x32_bf16 v[240:243], v[72:75], v[84:87], v[240:243]\n\t" \
  "buffer_load_dwordx4 v[36:39], v14, %[rs], s68 offen sc1\n\t" \
  "v_mfma_f32_16x16x32_bf16 v[200:203], v[72:75], v[80:83], v[200:203]\n\t" \
  "buffer_load_dwordx4 v[104:107], v15, %[rs], s68 offen sc1\n\t" \
  "v_mfma_f32_16x16x32_bf16 v[180:183], v[72:75], v[24:27], v[180:183]\n\t" \
  "buffer_load_dwordx4 v[108:111], %[vo], %[rs], s74 offen offset:0 sc1\n\t" \
  "v_mfma_f32_16x16x32_bf16 v[224:227], v[76:79], v[20:23], v[224:227]\n\t" \
  "buffer_load_dwordx4 v[112:115], %[vo], %[rs], s74 offen offset:1024 sc1\n\t" \
  "v_mfma_f32_16x16x32_bf16 v[244:247], v[76:79], v[84:87], v[244:247]\n\t" \
  "buffer_load_dwordx4 v[116:119], %[vo], %[rs], s74 offen offset:2048 sc1\n\t" \
  "v_mfma_f32_16x16x32_bf16 v[204:207], v[76:79], v[80:83], v[204:207]\n\t" \
  "v_bfe_u32 v11, %[vo], 4, 3\n\t" \
  "v_mfma_f32_16x16x32_bf16 v[184:187], v[76:79], v[24:27], v[184:187]\n\t" \
  "v_lshlrev_b32 v11, 7, v11\n\t" \
  "v_mfma_f32_16x16x32_bf16 v[228:231], v[28:31], v[20:23], v[228:231]\n\t" \
  "s_lshl_b32 %[cnt], %[w], 8\n\t" \
  "s_sub_u32 %[cnt], 32768, %[cnt]\n\t" \
  "v_add_u32 v11, %[cnt], v11\n\t" \
  "v_mfma_f32_16x16x32_bf16 v[248:251], v[28:31], v[84:87], v[248:251]\n\t" \
  "v_mfma_f32_16x16x32_bf16 v[208:211], v[28:31], v[80:83], v[208:211]\n\t" \
  "v_mfma_f32_16x16x32_bf16 v[188:191], v[28:31], v[24:27], v[188:191]\n\t" \
  "v_mfma_f32_16x16x32_bf16 v[232:235], v[32:35], v[20:23], v[232:235]\n\t" \
  "v_mfma_f32_16x16x32_bf16 v[252:255], v[32:35], v[84:87], v[252:255]\n\t" \
  "v_mfma_f32_16x16x32_bf16 v[212:215], v[32:35], v[80:83], v[212:215]\n\t" \
  "v_mfma_f32_16x16x32_bf16 v[192:195], v[32:35], v[24:27], v[192:195]\n\t" \
  "s_waitcnt vmcnt(3)\n\t" \
  "v_mfma_f32_16x16x32_bf16 v[216:219], v[88:91], v[36:39], v[216:219]\n\t" \
  "buffer_load_dwordx4 v[44:47], %[vo], %[rs], s74 offen offset:3072 sc1\n\t" \
  "v_mfma_f32_16x16x32_bf16 v[236:239], v[88:91], v[104:107], v[236:239]\n\t" \
  "buffer_load_dwordx4 v[172:175], v16, %[rs], s74 offen offset:0 sc1\n\t" \
  "v_mfma_f32_16x16x32_bf16 v[196:199], v[88:91], v[100:103], v[196:199]\n\t" \
  "buffer_load_dwordx4 v[144:147], v12, %[rs], s69 offen sc1\n\t" \
  "v_mfma_f32_16x16x32_bf16 v[176:179], v[88:91], v[152:155], v[176:179]\n\t" \
  "buffer_load_dwordx4 v[120:123], v13, %[rs], s69 offen sc1\n\t" \
  "v_mfma_f32_16x16x32_bf16 v[220:223], v[92:95], v[36:39], v[220:223]\n\t" \
  "buffer_load_dwordx4 v[40:43], v14, %[rs], s69 offen sc1\n\t" \
  "v_mfma_f32_16x16x32_bf16 v[240:243], v[92:95], v[104:107], v[240:243]\n\t" \
  "buffer_load_dwordx4 v[124:127], v15, %[rs], s69 offen sc1\n\t" \
  "v_mfma_f32_16x16x32_bf16 v[200:203], v[92:95], v[100:103], v[200:203]\n\t" \
  "buffer_load_dwordx4 v[128:131], v12, %[rs], s70 offen sc1\n\t" \
  "v_mfma_f32_16x16x32_bf16 v[180:183], v[92:95], v[152:155], v[180:183]\n\t" \
  "buffer_load_dwordx4 v[132:135], v13, %[rs], s70 offen sc1\n\t" \
  "v_mfma_f32_16x16x32_bf16 v[224:227], v[96:99], v[36:39], v[224:227]\n\t" \
  "buffer_load_dwordx4 v[136:139], v14, %[rs], s70 offen sc1\n\t" \
  "v_mfma_f32_16x16x32_bf16 v[244:247], v[96:99], v[104:107], v[244:247]\n\t" \
  "v_mfma_f32_16x16x32_bf16 v[204:207], v[96:99], v[100:103], v[204:207]\n\t" \
  "v_mfma_f32_16x16x32_bf16 v[184:187], v[96:99], v[152:155], v[184:187]\n\t" \
  "v_mfma_f32_16x16x32_bf16 v[228:231], v[156:159], v[36:39], v[228:231]\n\t" \
  "v_mfma_f32_16x16x32_bf16 v[248:251], v[156:159], v[104:107], v[248:251]\n\t" \
  "v_mfma_f32_16x16x32_bf16 v[208:211], v[156:159], v[100:103], v[208:211]\n\t" \
  "v_mfma_f32_16x16x32_bf16 v[188:191], v[156:159], v[152:155], v[188:191]\n\t" \
  "v_mfma_f32_16x16x32_bf16 v[232:235], v[148:151], v[36:39], v[232:235]\n\t" \
  "v_mfma_f32_16x16x32_bf16 v[252:255], v[148:151], v[104:107], v[252:255]\n\t" \
  "v_mfma_f32_16x16x32_bf16 v[212:215], v[148:151], v[100:103], v[212:215]\n\t" \
  "v_mfma_f32_16x16x32_bf16 v[192:195], v[148:151], v[152:155], v[192:195]\n\t" \
  "s_waitcnt vmcnt(3)\n\t" \
  "v_mfma_f32_16x16x32_bf16 v[216:219], v[108:111], v[40:43], v[216:219]\n\t" \
  "ds_read_b128 v[160:163], v10 offset:0\n\t" \
  "v_mfma_f32_16x16x32_bf16 v[236:239], v[108:111], v[124:127], v[236:239]\n\t" \
  "ds_read_b128 v[64:67], v10 offset:1024\n\t" \
  "v_mfma_f32_16x16x32_bf16 v[196:199], v[108:111], v[120:123], v[196:199]\n\t" \
  "ds_read_b128 v[60:63], v10 offset:2048\n\t" \
  "v_mfma_f32_16x16x32_bf16 v[176:179], v[108:111], v[144:147], v[176:179]\n\t" \
  "ds_read_b128 v[140:143], v10 offset:3072\n\t" \
  "v_mfma_f32_16x16x32_bf16 v[220:223], v[112:115], v[40:43], v[220:223]\n\t" \
  "ds_read_b128 v[164:167], v10 offset:4096\n\t" \
  "v_mfma_f32_16x16x32_bf16 v[240:243], v[112:115], v[124:127], v[240:243]\n\t" \
  "buffer_load_dwordx4 v[68:71], v15, %[rs], s70 offen sc1\n\t" \
  "v_mfma_f32_16x16x32_bf16 v[200:203], v[112:115], v[120:123], v[200:203]\n\t" \
  "buffer_load_dwordx4 v[72:75], v12, %[rs], s71 offen sc1\n\t" \
  "v_mfma_f32_16x16x32_bf16 v[180:183], v[112:115], v[144:147], v[180:183]\n\t" \
  "buffer_load_dwordx4 v[76:79], v13, %[rs], s71 offen sc1\n\t" \
  "v_mfma_f32_16x16x32_bf16 v[224:227], v[116:119], v[40:43], v[224:227]\n\t" \
  "buffer_load_dwordx4 v[28:31], v14, %[rs], s71 offen sc1\n\t" \
  "v_mfma_f32_16x16x32_bf16 v[244:247], v[116:119], v[124:127], v[244:247]\n\t" \
  "buffer_load_dwordx4 v[20:23], v15, %[rs], s71 offen sc1\n\t" \
  "v_mfma_f32_16x16x32_bf16 v[204:207], v[116:119], v[120:123], v[204:207]\n\t" \
  "v_mfma_f32_16x16x32_bf16 v[184:187], v[116:119], v[144:147], v[184:187]\n\t" \
  "v_mfma_f32_16x16x32_bf16 v[228:231], v[44:47], v[40:43], v[228:231]\n\t" \
  "v_mfma_f32_16x16x32_bf16 v[248:251], v[44:47], v[124:127], v[248:251]\n\t" \
  "v_mfma_f32_16x16x32_bf16 v[208:211], v[44:47], v[120:123], v[208:211]\n\t" \
  "v_mfma_f32_16x16x32_bf16 v[188:191], v[44:47], v[144:147], v[188:191]\n\t" \
  "v_mfma_f32_16x16x32_bf16 v[232:235], v[172:175], v[40:43], v[232:235]\n\t" \
  "v_mfma_f32_16x16x32_bf16 v[252:255], v[172:175], v[124:127], v[252:255]\n\t" \
  "v_mfma_f32_16x16x32_bf16 v[212:215], v[172:175], v[120:123], v[212:215]\n\t" \
  "v_mfma_f32_16x16x32_bf16 v[192:195], v[172:175], v[144:147], v[192:195]\n\t" \
  "s_waitcnt vmcnt(4)\n\t" \
  "s_waitcnt lgkmcnt(4)\n\t" \
  "v_mfma_f32_16x16x32_bf16 v[216:219], v[160:163], v[136:139], v[216:219]\n\t" \
  "ds_read_b128 v[116:119], v10 offset:5120\n\t" \
  "v_mfma_f32_16x16x32_bf16 v[236:239], v[160:163], v[68:71], v[236:239]\n\t" \
  "ds_read_b128 v[44:47], v10 offset:6144\n\t" \
  "v_mfma_f32_16x16x32_bf16 v[196:199], v[160:163], v[132:135], v[196:199]\n\t" \
  "ds_read_b128 v[40:43], v10 offset:7168\n\t" \
  "v_mfma_f32_16x16x32_bf16 v[176:179], v[160:163], v[128:131], v[176:179]\n\t" \
  "ds_read_b128 v[124:127], v10 offset:8192\n\t" \
  "s_waitcnt lgkmcnt(7)\n\t" \
  "v_mfma_f32_16x16x32_bf16 v[220:223], v[64:67], v[136:139], v[220:223]\n\t" \
  "ds_read_b128 v[120:123], v10 offset:9216\n\t" \
  "v_mfma_f32_16x16x32_bf16 v[240:243], v[64:67], v[68:71], v[240:243]\n\t" \
  "buffer_load_dword v17, v17, %[rs], %[go] offen sc1\n\t" \
  "v_mfma_f32_16x16x32_bf16 v[200:203], v[64:67], v[132:135], v[200:203]\n\t" \
  "buffer_load_dwordx2 v[8:9], v11, %[rs], %[f1B] offen sc1\n\t" \
  "v_mfma_f32_16x16x32_bf16 v[180:183], v[64:67], v[128:131], v[180:183]\n\t" \
  "ds_read_b128 v[172:175], v48 offset:0\n\t" \
  "s_waitcnt lgkmcnt(8)\n\t" \
  "v_mfma_f32_16x16x32_bf16 v[224:227], v[60:63], v[136:139], v[224:227]\n\t" \
  "ds_read_b128 v[144:147], v48 offset:1024\n\t" \
  "v_mfma_f32_16x16x32_bf16 v[244:247], v[60:63], v[68:71], v[244:247]\n\t" \
  "ds_read_b128 v[160:163], v48 offset:2048\n\t" \
  "v_mfma_f32_16x16x32_bf16 v[204:207], v[60:63], v[132:135], v[204:207]\n\t" \
  "ds_read_b128 v[64:67], v48 offset:3072\n\t" \
  "v_mfma_f32_16x16x32_bf16 v[184:187], v[60:63], v[128:131], v[184:187]\n\t" \
  "ds_read_b128 v[60:63], v48 offset:4096\n\t" \
  "s_waitcnt lgkmcnt(11)\n\t" \
  "v_mfma_f32_16x16x32_bf16 v[228:231], v[140:143], v[136:139], v[228:231]\n\t" \
  "v_mfma_f32_16x16x32_bf16 v[248:251], v[140:143], v[68:71], v[248:251]\n\t" \
  "v_mfma_f32_16x16x32_bf16 v[208:211], v[140:143], v[132:135], v[208:211]\n\t" \
  "v_mfma_f32_16x16x32_bf16 v[188:191], v[140:143], v[128:131], v[188:191]\n\t" \
  "ds_read_b128 v[140:143], v48 offset:5120\n\t" \
  "s_waitcnt lgkmcnt(11)\n\t" \
  "v_mfma_f32_16x16x32_bf16 v[232:235], v[164:167], v[136:139], v[232:235]\n\t" \
  "ds_read_b128 v[136:139], v48 offset:6144\n\t" \
  "v_mfma_f32_16x16x32_bf16 v[252:255], v[164:167], v[68:71], v[252:255]\n\t" \
  "ds_read_b128 v[68:71], v48 offset:7168\n\t" \
  "v_mfma_f32_16x16x32_bf16 v[212:215], v[164:167], v[132:135], v[212:215]\n\t" \
  "ds_read_b128 v[132:135], v48 offset:8192\n\t" \
  "v_mfma_f32_16x16x32_bf16 v[192:195], v[164:167], v[128:131], v[192:195]\n\t" \
  "ds_read_b128 v[164:167], v48 offset:9216\n\t" \
  "s_waitcnt vmcnt(2)\n\t" \
  "s_waitcnt lgkmcnt(14)\n\t" \
  "v_mfma_f32_16x16x32_bf16 v[216:219], v[116:119], v[28:31], v[216:219]\n\t" \
  "ds_read_b128 v[128:131], v48 offset:10240\n\t" \
  "v_mfma_f32_16x16x32_bf16 v[236:239], v[116:119], v[20:23], v[236:239]\n\t" \
  "v_mfma_f32_16x16x32_bf16 v[196:199], v[116:119], v[76:79], v[196:199]\n\t" \
  "v_mfma_f32_16x16x32_bf16 v[176:179], v[116:119], v[72:75], v[176:179]\n\t" \
  "ds_read_b128 v[116:119], v48 offset:11264\n\t" \
  "s_waitcnt lgkmcnt(14)\n\t" \
  "v_mfma_f32_16x16x32_bf16 v[220:223], v[44:47], v[28:31], v[220:223]\n\t" \
  "v_mfma_f32_16x16x32_bf16 v[240:243], v[44:47], v[20:23], v[240:243]\n\t" \
  "v_mfma_f32_16x16x32_bf16 v[200:203], v[44:47], v[76:79], v[200:203]\n\t" \
  "v_mfma_f32_16x16x32_bf16 v[180:183], v[44:47], v[72:75], v[180:183]\n\t" \
  "ds_read_b128 v[44:47], v48 offset:12288\n\t" \
  "v_mfma_f32_16x16x32_bf16 v[224:227], v[40:43], v[28:31], v[224:227]\n\t" \
  "v_mfma_f32_16x16x32_bf16 v[244:247], v[40:43], v[20:23], v[244:247]\n\t" \
  "v_mfma_f32_16x16x32_bf16 v[204:207], v[40:43], v[76:79], v[204:207]\n\t" \
  "v_mfma_f32_16x16x32_bf16 v[184:187], v[40:43], v[72:75], v[184:187]\n\t" \
  "ds_read_b128 v[40:43], v48 offset:13312\n\t" \
  "s_waitcnt vmcnt(0)\n\t" \
  "v_cmp_eq_u64 vcc, %[tag], v[8:9]\n\t" \
  "s_cmp_eq_u64 vcc, exec\n\t" \
  "s_cselect_b32 %[rdy], 1, 0\n\t" \
  "s_cbranch_scc0 P2N_%=\n\t" \
  "buffer_load_dwordx4 v[84:87], v12, %[rs], s76 offen sc1\n\t" \
  "buffer_load_dwordx4 v[80:83], v12, %[rs], s77 offen sc1\n\t" \
  "buffer_load_dwordx4 v[32:35], v12, %[rs], s78 offen sc1\n\t" \
  "buffer_load_dwordx4 v[24:27], v12, %[rs], s79 offen sc1\n\t" \
  "buffer_load_dwordx4 v[88:91], v12, %[rs], s80 offen sc1\n\t" \
  "buffer_load_dwordx4 v[92:95], v12, %[rs], s81 offen sc1\n\t" \
  "buffer_load_dwordx4 v[96:99], v12, %[rs], s82 offen sc1\n\t" \
  "buffer_load_dwordx4 v[156:159], v12, %[rs], s83 offen sc1\n\t" \
  "P2N_%=:\n\t" \
  "s_waitcnt lgkmcnt(14)\n\t" \
  "v_mfma_f32_16x16x32_bf16 v[228:231], v[124:127], v[28:31], v[228:231]\n\t" \
  "v_mfma_f32_16x16x32_bf16 v[248:251], v[124:127], v[20:23], v[248:251]\n\t" \
  "v_mfma_f32_16x16x32_bf16 v[208:211], v[124:127], v[76:79], v[208:211]\n\t" \
  "v_mfma_f32_16x16x32_bf16 v[188:191], v[124:127], v[72:75], v[188:191]\n\t" \
  "ds_read_b128 v[124:127], v48 offset:14336\n\t" \
  "v_mfma_f32_16x16x32_bf16 v[232:235], v[120:123], v[28:31], v[232:235]\n\t" \
  "ds_read_b128 v[28:31], v48 offset:15360\n\t" \
  "v_mfma_f32_16x16x32_bf16 v[252:255], v[120:123], v[20:23], v[252:255]\n\t" \
  "ds_read_b128 v[20:23], v48 offset:16384\n\t" \
  "v_mfma_f32_16x16x32_bf16 v[212:215], v[120:123], v[76:79], v[212:215]\n\t" \
  "ds_read_b128 v[76:79], v48 offset:17408\n\t" \
  "v_mfma_f32_16x16x32_bf16 v[192:195], v[120:123], v[72:75], v[192:195]\n\t" \
  "ds_read_b128 v[120:123], v48 offset:18432\n\t" \
  "s_nop 1\n\t" \
  "ds_write_b128 v10, v[216:219] offset:0\n\t" \
  "ds_write_b128 v10, v[220:223] offset:1024\n\t" \
  "ds_write_b128 v10, v[224:227] offset:2048\n\t" \
  "ds_write_b128 v10, v[228:231] offset:3072\n\t" \
  "s_cmp_lg_u32 %[rdy], 0\n\t" \
  "s_cbranch_scc1 P2R_%=\n\t" \
  "v_cmp_eq_u64 vcc, %[tag], v[8:9]\n\t" \
  "s_cmp_eq_u64 vcc, exec\n\t" \
  "s_cbranch_scc1 P2K3_%=\n\t" \
  "s_mov_b32 %[cnt], 0x10000\n\t" \
  "P2L3_%=:\n\t" \
  "s_sleep 1\n\t" \
  "buffer_load_dwordx2 v[8:9], v11, %[rs], %[f1B] offen sc1\n\t" \
  "s_sub_u32 %[cnt], %[cnt], 1\n\t" \
  "s_waitcnt vmcnt(0)\n\t" \
  "v_cmp_eq_u64 vcc, %[tag], v[8:9]\n\t" \
  "s_cmp_eq_u64 vcc, exec\n\t" \
  "s_cbranch_scc1 P2K3_%=\n\t" \
  "s_cmp_lg_u32 %[cnt], 0\n\t" \
  "s_cbranch_scc1 P2L3_%=\n\t" \
  "P2K3_%=:\n\t" \
  "buffer_load_dwordx4 v[84:87], v12, %[rs], s76 offen sc1\n\t" \
  "buffer_load_dwordx4 v[80:83], v12, %[rs], s77 offen sc1\n\t" \
  "buffer_load_dwordx4 v[32:35], v12, %[rs], s78 offen sc1\n\t" \
  "buffer_load_dwordx4 v[24:27], v12, %[rs], s79 offen sc1\n\t" \
  "buffer_load_dwordx4 v[88:91], v12, %[rs], s80 offen sc1\n\t" \
  "buffer_load_dwordx4 v[92:95], v12, %[rs], s81 offen sc1\n\t" \
  "buffer_load_dwordx4 v[96:99], v12, %[rs], s82 offen sc1\n\t" \
  "buffer_load_dwordx4 v[156:159], v12, %[rs], s83 offen sc1\n\t" \
  "P2R_%=:\n\t" \
  "s_waitcnt vmcnt(7)\n\t" \
  "s_waitcnt lgkmcnt(14)\n\t" \
  "v_mfma_f32_16x16x32_bf16 v[100:103], v[172:175], v[84:87], 0\n\t" \
  "ds_read_b128 v[72:75], v48 offset:19456\n\t" \
  "buffer_load_dwordx4 v[36:39], v12, %[rs], s84 offen sc1\n\t" \
  "v_mfma_f32_16x16x32_bf16 v[148:151], v[144:147], v[84:87], 0\n\t" \
  "ds_read_b128 v[172:175], v48 offset:20480\n\t" \
  "ds_write_b128 v10, v[232:235] offset:4096\n\t" \
  "v_mfma_f32_16x16x32_bf16 v[152:155], v[160:163], v[84:87], 0\n\t" \
  "ds_read_b128 v[144:147], v48 offset:21504\n\t" \
  "ds_write_b128 v10, v[236:239] offset:5120\n\t" \
  "v_mfma_f32_16x16x32_bf16 v[108:111], v[64:67], v[84:87], 0\n\t" \
  "ds_read_b128 v[160:163], v48 offset:22528\n\t" \
  "ds_write_b128 v10, v[240:243] offset:6144\n\t" \
  "v_mfma_f32_16x16x32_bf16 v[112:115], v[60:63], v[84:87], 0\n\t" \
  "ds_read_b128 v[64:67], v48 offset:23552\n\t" \
  "ds_write_b128 v10, v[244:247] offset:7168\n\t" \
  "s_waitcnt vmcnt(7)\n\t" \
  "v_mfma_f32_16x16x32_bf16 v[100:103], v[140:143], v[80:83], v[100:103]\n\t" \
  "ds_read_b128 v[60:63], v48 offset:24576\n\t" \
  "buffer_load_dwordx4 v[104:107], v12, %[rs], s85 offen sc1\n\t" \
  "v_mfma_f32_16x16x32_bf16 v[148:151], v[136:139], v[80:83], v[148:151]\n\t" \
  "ds_read_b128 v[140:143], v48 offset:25600\n\t" \
  "ds_write_b128 v10, v[248:251] offset:8192\n\t" \
  "v_mfma_f32_16x16x32_bf16 v[152:155], v[68:71], v[80:83], v[152:155]\n\t" \
  "ds_read_b128 v[136:139], v48 offset:26624\n\t" \
  "ds_write_b128 v10, v[252:255] offset:9216\n\t" \
  "v_mfma_f32_16x16x32_bf16 v[108:111], v[132:135], v[80:83], v[108:111]\n\t" \
  "ds_read_b128 v[68:71], v48 offset:27648\n\t" \
  "s_mov_b64 exec, 1\n\t" \
  "ds_add_u32 v52, v19 offset:0\n\t" \
  "s_mov_b64 exec, -1\n\t" \
  "s_waitcnt lgkmcnt(14)\n\t" \
  "v_mfma_f32_16x16x32_bf16 v[112:115], v[164:167], v[80:83], v[112:115]\n\t" \
  "ds_read_b128 v[132:135], v48 offset:28672\n\t" \
  "s_waitcnt vmcnt(7)\n\t" \
  "v_mfma_f32_16x16x32_bf16 v[100:103], v[128:131], v[32:35], v[100:103]\n\t" \
  "ds_read_b128 v[164:167], v48 offset:29696\n\t" \
  "buffer_load_dwordx4 v[84:87], v12, %[rs], s86 offen sc1\n\t" \
  "v_mfma_f32_16x16x32_bf16 v[148:151], v[116:119], v[32:35], v[148:151]\n\t" \
  "ds_read_b128 v[128:131], v48 offset:30720\n\t" \
  "v_mfma_f32_16x16x32_bf16 v[152:155], v[44:47], v[32:35], v[152:155]\n\t" \
  "ds_read_b128 v[116:119], v48 offset:31744\n\t" \
  "v_mfma_f32_16x16x32_bf16 v[108:111], v[40:43], v[32:35], v[108:111]\n\t" \
  "ds_read_b128 v[44:47], v48 offset:32768\n\t" \
  "v_mfma_f32_16x16x32_bf16 v[112:115], v[124:127], v[32:35], v[112:115]\n\t" \
  "ds_read_b128 v[40:43], v48 offset:33792\n\t" \
  "s_waitcnt vmcnt(7)\n\t" \
  "v_mfma_f32_16x16x32_bf16 v[100:103], v[28:31], v[24:27], v[100:103]\n\t" \
  "ds_read_b128 v[124:127], v48 offset:34816\n\t" \
  "buffer_load_dwordx4 v[80:83], v12, %[rs], s87 offen sc1\n\t" \
  "v_mfma_f32_16x16x32_bf16 v[148:151], v[20:23], v[24:27], v[148:151]\n\t" \
  "ds_read_b128 v[28:31], v48 offset:35840\n\t" \
  "v_mfma_f32_16x16x32_bf16 v[152:155], v[76:79], v[24:27], v[152:155]\n\t" \
  "ds_read_b128 v[20:23], v48 offset:36864\n\t" \
  "v_mfma_f32_16x16x32_bf16 v[108:111], v[120:123], v[24:27], v[108:111]\n\t" \
  "ds_read_b128 v[76:79], v48 offset:37888\n\t" \
  "v_mfma_f32_16x16x32_bf16 v[112:115], v[72:75], v[24:27], v[112:115]\n\t" \
  "ds_read_b128 v[120:123], v48 offset:38912\n\t" \
  "ds_read_b32 v8, v52 offset:0\n\t" \
  "s_waitcnt vmcnt(7)\n\t" \
  "v_mfma_f32_16x16x32_bf16 v[100:103], v[172:175], v[88:91], v[100:103]\n\t" \
  "ds_read_b128 v[72:75], v48 offset:39936\n\t" \
  "buffer_load_dwordx4 v[32:35], v12, %[rs], s88 offen sc1\n\t" \
  "s_waitcnt lgkmcnt(14)\n\t" \
  "v_mfma_f32_16x16x32_bf16 v[148:151], v[144:147], v[88:91], v[148:151]\n\t" \
  "ds_read_b128 v[172:175], v48 offset:40960\n\t" \
  "v_mfma_f32_16x16x32_bf16 v[152:155], v[160:163], v[88:91], v[152:155]\n\t" \
  "ds_read_b128 v[144:147], v48 offset:41984\n\t" \
  "v_mfma_f32_16x16x32_bf16 v[108:111], v[64:67], v[88:91], v[108:111]\n\t" \
  "ds_read_b128 v[160:163], v48 offset:43008\n\t" \
  "s_waitcnt lgkmcnt(4)\n\t" \
  "v_cmp_le_u32 vcc, 2, v8\n\t" \
  "s_cmp_eq_u64 vcc, exec\n\t" \
  "s_cbranch_scc1 P2K4_%=\n\t" \
  "s_mov_b32 %[cnt], 0x400000\n\t" \
  "P2L4_%=:\n\t" \
  "ds_read_b32 v8, v52 offset:0\n\t" \
  "s_sub_u32 %[cnt], %[cnt], 1\n\t" \
  "s_waitcnt lgkmcnt(0)\n\t" \
  "v_cmp_le_u32 vcc, 2, v8\n\t" \
  "s_cmp_eq_u64 vcc, exec\n\t" \
  "s_cbranch_scc1 P2K4_%=\n\t" \
  "s_cmp_lg_u32 %[cnt], 0\n\t" \
  "s_cbranch_scc1 P2L4_%=\n\t" \
  "P2K4_%=:\n\t" \
  "v_mfma_f32_16x16x32_bf16 v[112:115], v[60:63], v[88:91], v[112:115]\n\t" \
  "ds_read_b128 v[64:67], v48 offset:44032\n\t" \
  "ds_read_b128 v[236:239], v50 offset:5120\n\t" \
  "s_waitcnt vmcnt(7)\n\t" \
  "v_mfma_f32_16x16x32_bf16 v[100:103], v[140:143], v[92:95], v[100:103]\n\t" \
  "ds_read_b128 v[60:63], v48 offset:45056\n\t" \
  "buffer_load_dwordx4 v[24:27], v12, %[rs], s89 offen sc1\n\t" \
  "v_mfma_f32_16x16x32_bf16 v[148:151], v[136:139], v[92:95], v[148:151]\n\t" \
  "ds_read_b128 v[140:143], v48 offset:46080\n\t" \
  "ds_read_b128 v[240:243], v50 offset:6144\n\t" \
  "v_mfma_f32_16x16x32_bf16 v[152:155], v[68:71], v[92:95], v[152:155]\n\t" \
  "ds_read_b128 v[136:139], v48 offset:47104\n\t" \
  "ds_read_b128 v[244:247], v50 offset:7168\n\t" \
  "v_mfma_f32_16x16x32_bf16 v[108:111], v[132:135], v[92:95], v[108:111]\n\t" \
  "ds_read_b128 v[68:71], v48 offset:48128\n\t" \
  "ds_read_b128 v[248:251], v50 offset:8192\n\t" \
  "v_mfma_f32_16x16x32_bf16 v[112:115], v[164:167], v[92:95], v[112:115]\n\t" \
  "ds_read_b128 v[132:135], v48 offset:49152\n\t" \
  "ds_read_b128 v[252:255], v50 offset:9216\n\t" \
  "s_waitcnt vmcnt(7)\n\t" \
  "v_mfma_f32_16x16x32_bf16 v[100:103], v[128:131], v[96:99], v[100:103]\n\t" \
  "ds_read_b128 v[164:167], v48 offset:50176\n\t" \
  "buffer_load_dwordx4 v[88:91], v12, %[rs], s90 offen sc1\n\t" \
  "v_mfma_f32_16x16x32_bf16 v[148:151], v[116:119], v[96:99], v[148:151]\n\t" \
  "ds_read_b128 v[128:131], v48 offset:51200\n\t" \
  "ds_read_b128 v[216:219], v50 offset:0\n\t" \
  "v_mfma_f32_16x16x32_bf16 v[152:155], v[44:47], v[96:99], v[152:155]\n\t" \
  "ds_read_b128 v[116:119], v48 offset:52224\n\t" \
  "ds_read_b128 v[220:223], v50 offset:1024\n\t" \
  "v_mfma_f32_16x16x32_bf16 v[108:111], v[40:43], v[96:99], v[108:111]\n\t" \
  "ds_read_b128 v[44:47], v48 offset:53248\n\t" \
  "ds_read_b128 v[224:227], v50 offset:2048\n\t" \
  "v_mfma_f32_16x16x32_bf16 v[112:115], v[124:127], v[96:99], v[112:115]\n\t" \
  "ds_read_b128 v[40:43], v48 offset:54272\n\t" \
  "ds_read_b128 v[228:231], v50 offset:3072\n\t" \
  "s_waitcnt vmcnt(7)\n\t" \
  "v_mfma_f32_16x16x32_bf16 v[100:103], v[28:31], v[156:159], v[100:103]\n\t" \
  "ds_read_b128 v[124:127], v48 offset:55296\n\t" \
  "buffer_load_dwordx4 v[92:95], v12, %[rs], s91 offen sc1\n\t" \
  "v_mfma_f32_16x16x32_bf16 v[148:151], v[20:23], v[156:159], v[148:151]\n\t" \
  "ds_read_b128 v[28:31], v48 offset:56320\n\t" \
  "ds_read_b128 v[232:235], v50 offset:4096\n\t" \
  "v_mfma_f32_16x16x32_bf16 v[152:155], v[76:79], v[156:159], v[152:155]\n\t" \
  "ds_read_b128 v[20:23], v48 offset:57344\n\t" \
  "s_waitcnt lgkmcnt(14)\n\t" \
  "v_add_f32 v196, v196, v236\n\t" \
  "v_mfma_f32_16x16x32_bf16 v[108:111], v[120:123], v[156:159], v[108:111]\n\t" \
  "ds_read_b128 v[76:79], v48 offset:58368\n\t" \
  "v_add_f32 v197, v197, v237\n\t" \
  "v_mfma_f32_16x16x32_bf16 v[112:115], v[72:75], v[156:159], v[112:115]\n\t" \
  "ds_read_b128 v[120:123], v48 offset:59392\n\t" \
  "v_add_f32 v198, v198, v238\n\t" \
  "s_waitcnt vmcnt(7)\n\t" \
  "v_mfma_f32_16x16x32_bf16 v[100:103], v[172:175], v[36:39], v[100:103]\n\t" \
  "ds_read_b128 v[72:75], v48 offset:60416\n\t" \
  "buffer_load_dwordx4 v[96:99], v12, %[rs], s92 offen sc1\n\t" \
  "v_mfma_f32_16x16x32_bf16 v[148:151], v[144:147], v[36:39], v[148:151]\n\t" \
  "ds_read_b128 v[172:175], v49 offset:0\n\t" \
  "v_add_f32 v199, v199, v239\n\t" \
  "v_mfma_f32_16x16x32_bf16 v[152:155], v[160:163], v[36:39], v[152:155]\n\t" \
  "ds_read_b128 v[144:147], v49 offset:1024\n\t" \
  "ds_write_b128 v50, v[196:199] offset:5120\n\t" \
  "v_mfma_f32_16x16x32_bf16 v[108:111], v[64:67], v[36:39], v[108:111]\n\t" \
  "ds_read_b128 v[160:163], v49 offset:2048\n\t" \
  "v_add_f32 v200, v200, v240\n\t" \
  "v_mfma_f32_16x16x32_bf16 v[112:115], v[60:63], v[36:39], v[112:115]\n\t" \
  "ds_read_b128 v[64:67], v49 offset:3072\n\t" \
  "v_add_f32 v201, v201, v241\n\t" \
  "s_waitcnt vmcnt(7)\n\t" \
  "v_mfma_f32_16x16x32_bf16 v[100:103], v[140:143], v[104:107], v[100:103]\n\t" \
  "ds_read_b128 v[60:63], v49 offset:4096\n\t" \
  "buffer_load_dwordx4 v[156:159], v12, %[rs], s93 offen sc1\n\t" \
  "v_mfma_f32_16x16x32_bf16 v[148:151], v[136:139], v[104:107], v[148:151]\n\t" \
  "ds_read_b128 v[140:143], v49 offset:5120\n\t" \
  "v_add_f32 v202, v202, v242\n\t" \
  "v_mfma_f32_16x16x32_bf16 v[152:155], v[68:71], v[104:107], v[152:155]\n\t" \
  "ds_read_b128 v[136:139], v49 offset:6144\n\t" \
  "v_add_f32 v203, v203, v243\n\t" \
  "v_mfma_f32_16x16x32_bf16 v[108:111], v[132:135], v[104:107], v[108:111]\n\t" \
  "ds_read_b128 v[68:71], v49 offset:7168\n\t" \
  "ds_write_b128 v50, v[200:203] offset:6144\n\t" \
  "s_waitcnt lgkmcnt(14)\n\t" \
  "v_mfma_f32_16x16x32_bf16 v[112:115], v[164:167], v[104:107], v[112:115]\n\t" \
  "ds_read_b128 v[132:135], v49 offset:8192\n\t" \
  "v_add_f32 v204, v204, v244\n\t" \
  "s_waitcnt vmcnt(7)\n\t" \
  "v_mfma_f32_16x16x32_bf16 v[100:103], v[128:131], v[84:87], v[100:103]\n\t" \
  "ds_read_b128 v[164:167], v49 offset:9216\n\t" \
  "buffer_load_dwordx4 v[36:39], v12, %[rs], s94 offen sc1\n\t" \
  "v_mfma_f32_16x16x32_bf16 v[148:151], v[116:119], v[84:87], v[148:151]\n\t" \
  "ds_read_b128 v[128:131], v49 offset:10240\n\t" \
  "v_add_f32 v205, v205, v245\n\t" \
  "v_mfma_f32_16x16x32_bf16 v[152:155], v[44:47], v[84:87], v[152:155]\n\t" \
  "ds_read_b128 v[116:119], v49 offset:11264\n\t" \
  "v_add_f32 v206, v206, v246\n\t" \
  "v_mfma_f32_16x16x32_bf16 v[108:111], v[40:43], v[84:87], v[108:111]\n\t" \
  "ds_read_b128 v[44:47], v49 offset:12288\n\t" \
  "v_add_f32 v207, v207, v247\n\t" \
  "v_mfma_f32_16x16x32_bf16 v[112:115], v[124:127], v[84:87], v[112:115]\n\t" \
  "ds_read_b128 v[40:43], v49 offset:13312\n\t" \
  "ds_write_b128 v50, v[204:207] offset:7168\n\t" \
  "s_waitcnt vmcnt(7)\n\t" \
  "v_mfma_f32_16x16x32_bf16 v[100:103], v[28:31], v[80:83], v[100:103]\n\t" \
  "ds_read_b128 v[124:127], v49 offset:14336\n\t" \
  "buffer_load_dwordx4 v[104:107], v12, %[rs], s95 offen sc1\n\t" \
  "s_waitcnt lgkmcnt(14)\n\t" \
  "v_mfma_f32_16x16x32_bf16 v[148:151], v[20:23], v[80:83], v[148:151]\n\t" \
  "ds_read_b128 v[28:31], v49 offset:15360\n\t" \
  "v_add_f32 v208, v208, v248\n\t" \
  "v_mfma_f32_16x16x32_bf16 v[152:155], v[76:79], v[80:83], v[152:155]\n\t" \
  "ds_read_b128 v[20:23], v49 offset:16384\n\t" \
  "v_add_f32 v209, v209, v249\n\t" \
  "v_mfma_f32_16x16x32_bf16 v[108:111], v[120:123], v[80:83], v[108:111]\n\t" \
  "ds_read_b128 v[76:79], v49 offset:17408\n\t" \
  "v_add_f32 v210, v210, v250\n\t" \
  "v_mfma_f32_16x16x32_bf16 v[112:115], v[72:75], v[80:83], v[112:115]\n\t" \
  "ds_read_b128 v[120:123], v49 offset:18432\n\t" \
  "v_add_f32 v211, v211, v251\n\t" \
  "s_waitcnt vmcnt(7)\n\t" \
  "v_mfma_f32_16x16x32_bf16 v[100:103], v[172:175], v[32:35], v[100:103]\n\t" \
  "ds_read_b128 v[72:75], v49 offset:19456\n\t" \
  "ds_write_b128 v50, v[208:211] offset:8192\n\t" \
  "v_mfma_f32_16x16x32_bf16 v[148:151], v[144:147], v[32:35], v[148:151]\n\t" \
  "ds_read_b128 v[172:175], v49 offset:20480\n\t" \
  "v_add_f32 v212, v212, v252\n\t" \
  "v_mfma_f32_16x16x32_bf16 v[152:155], v[160:163], v[32:35], v[152:155]\n\t" \
  "ds_read_b128 v[144:147], v49 offset:21504\n\t" \
  "v_add_f32 v213, v213, v253\n\t" \
  "s_waitcnt lgkmcnt(14)\n\t" \
  "v_mfma_f32_16x16x32_bf16 v[108:111], v[64:67], v[32:35], v[108:111]\n\t" \
  "ds_read_b128 v[160:163], v49 offset:22528\n\t" \
  "v_add_f32 v214, v214, v254\n\t" \
  "v_mfma_f32_16x16x32_bf16 v[112:115], v[60:63], v[32:35], v[112:115]\n\t" \
  "ds_read_b128 v[64:67], v49 offset:23552\n\t" \
  "v_add_f32 v215, v215, v255\n\t" \
  "s_waitcnt vmcnt(6)\n\t" \
  "v_mfma_f32_16x16x32_bf16 v[100:103], v[140:143], v[24:27], v[100:103]\n\t" \
  "ds_read_b128 v[60:63], v49 offset:24576\n\t" \
  "ds_write_b128 v50, v[212:215] offset:9216\n\t" \
  "v_mfma_f32_16x16x32_bf16 v[148:151], v[136:139], v[24:27], v[148:151]\n\t" \
  "ds_read_b128 v[140:143], v49 offset:25600\n\t" \
  "s_mov_b64 exec, 1\n\t" \
  "ds_add_u32 v53, v19 offset:8\n\t" \
  "s_mov_b64 exec, -1\n\t" \
  "v_mfma_f32_16x16x32_bf16 v[152:155], v[68:71], v[24:27], v[152:155]\n\t" \
  "ds_read_b128 v[136:139], v49 offset:26624\n\t" \
  "v_add_f32 v176, v176, v216\n\t" \
  "v_mfma_f32_16x16x32_bf16 v[108:111], v[132:135], v[24:27], v[108:111]\n\t" \
  "ds_read_b128 v[68:71], v49 offset:27648\n\t" \
  "v_add_f32 v177, v177, v217\n\t" \
  "v_mfma_f32_16x16x32_bf16 v[112:115], v[164:167], v[24:27], v[112:115]\n\t" \
  "ds_read_b128 v[132:135], v49 offset:28672\n\t" \
  "v_add_f32 v178, v178, v218\n\t" \
  "s_waitcnt vmcnt(5)\n\t" \
  "s_waitcnt lgkmcnt(14)\n\t" \
  "v_mfma_f32_16x16x32_bf16 v[100:103], v[128:131], v[88:91], v[100:103]\n\t" \
  "ds_read_b128 v[164:167], v49 offset:29696\n\t" \
  "v_add_f32 v179, v179, v219\n\t" \
  "v_mfma_f32_16x16x32_bf16 v[148:151], v[116:119], v[88:91], v[148:151]\n\t" \
  "ds_read_b128 v[128:131], v49 offset:30720\n\t" \
  "v_add_f32 v180, v180, v220\n\t" \
  "v_mfma_f32_16x16x32_bf16 v[152:155], v[44:47], v[88:91], v[152:155]\n\t" \
  "ds_read_b128 v[116:119], v49 offset:31744\n\t" \
  "v_add_f32 v181, v181, v221\n\t" \
  "v_mfma_f32_16x16x32_bf16 v[108:111], v[40:43], v[88:91], v[108:111]\n\t" \
  "ds_read_b128 v[44:47], v49 offset:32768\n\t" \
  "v_add_f32 v182, v182, v222\n\t" \
  "v_mfma_f32_16x16x32_bf16 v[112:115], v[124:127], v[88:91], v[112:115]\n\t" \
  "ds_read_b128 v[40:43], v49 offset:33792\n\t" \
  "v_add_f32 v183, v183, v223\n\t" \
  "s_waitcnt vmcnt(4)\n\t" \
  "v_mfma_f32_16x16x32_bf16 v[100:103], v[28:31], v[92:95], v[100:103]\n\t" \
  "ds_read_b128 v[124:127], v49 offset:34816\n\t" \
  "v_add_f32 v184, v184, v224\n\t" \
  "v_mfma_f32_16x16x32_bf16 v[148:151], v[20:23], v[92:95], v[148:151]\n\t" \
  "ds_read_b128 v[28:31], v49 offset:35840\n\t" \
  "v_add_f32 v185, v185, v225\n\t" \
  "v_mfma_f32_16x16x32_bf16 v[152:155], v[76:79], v[92:95], v[152:155]\n\t" \
  "ds_read_b128 v[20:23], v49 offset:36864\n\t" \
  "v_add_f32 v186, v186, v226\n\t" \
  "s_waitcnt lgkmcnt(14)\n\t" \
  "v_mfma_f32_16x16x32_bf16 v[108:111], v[120:123], v[92:95], v[108:111]\n\t" \
  "ds_read_b128 v[76:79], v49 offset:37888\n\t" \
  "v_add_f32 v187, v187, v227\n\t" \
  "v_mfma_f32_16x16x32_bf16 v[112:115], v[72:75], v[92:95], v[112:115]\n\t" \
  "ds_read_b128 v[120:123], v49 offset:38912\n\t" \
  "v_add_f32 v188, v188, v228\n\t" \
  "s_waitcnt vmcnt(3)\n\t" \
  "v_mfma_f32_16x16x32_bf16 v[100:103], v[172:175], v[96:99], v[100:103]\n\t" \
  "ds_read_b128 v[72:75], v49 offset:39936\n\t" \
  "v_add_f32 v189, v189, v229\n\t" \
  "v_mfma_f32_16x16x32_bf16 v[148:151], v[144:147], v[96:99], v[148:151]\n\t" \
  "v_add_f32 v190, v190, v230\n\t" \
  "v_add_f32 v191, v191, v231\n\t" \
  "v_mfma_f32_16x16x32_bf16 v[152:155], v[160:163], v[96:99], v[152:155]\n\t" \
  "v_add_f32 v192, v192, v232\n\t" \
  "v_add_f32 v193, v193, v233\n\t" \
  "v_mfma_f32_16x16x32_bf16 v[108:111], v[64:67], v[96:99], v[108:111]\n\t" \
  "v_add_f32 v194, v194, v234\n\t" \
  "v_add_f32 v195, v195, v235\n\t" \
  "v_mfma_f32_16x16x32_bf16 v[112:115], v[60:63], v[96:99], v[112:115]\n\t" \
  "v_sub_f32 v17, 1.0, v17\n\t" \
  "ds_read_b32 v8, v53 offset:8\n\t" \
  "s_waitcnt vmcnt(2)\n\t" \
  "s_waitcnt lgkmcnt(14)\n\t" \
  "v_mfma_f32_16x16x32_bf16 v[100:103], v[140:143], v[156:159], v[100:103]\n\t" \
  "s_waitcnt lgkmcnt(0)\n\t" \
  "v_cmp_le_u32 vcc, 2, v8\n\t" \
  "s_cmp_eq_u64 vcc, exec\n\t" \
  "s_cbranch_scc1 P2K5_%=\n\t" \
  "s_mov_b32 %[cnt], 0x400000\n\t" \
  "P2L5_%=:\n\t" \
  "ds_read_b32 v8, v53 offset:8\n\t" \
  "s_sub_u32 %[cnt], %[cnt], 1\n\t" \
  "s_waitcnt lgkmcnt(0)\n\t" \
  "v_cmp_le_u32 vcc, 2, v8\n\t" \
  "s_cmp_eq_u64 vcc, exec\n\t" \
  "s_cbranch_scc1 P2K5_%=\n\t" \
  "s_cmp_lg_u32 %[cnt], 0\n\t" \
  "s_cbranch_scc1 P2L5_%=\n\t" \
  "P2K5_%=:\n\t" \
  "ds_read_b128 v[236:239], v51 offset:5120\n\t" \
  "v_mfma_f32_16x16x32_bf16 v[148:151], v[136:139], v[156:159], v[148:151]\n\t" \
  "ds_read_b128 v[240:243], v51 offset:6144\n\t" \
  "ds_read_b128 v[244:247], v51 offset:7168\n\t" \
  "v_mfma_f32_16x16x32_bf16 v[152:155], v[68:71], v[156:159], v[152:155]\n\t" \
  "ds_read_b128 v[248:251], v51 offset:8192\n\t" \
  "ds_read_b128 v[252:255], v51 offset:9216\n\t" \
  "v_mfma_f32_16x16x32_bf16 v[108:111], v[132:135], v[156:159], v[108:111]\n\t" \
  "s_waitcnt lgkmcnt(4)\n\t" \
  "v_add_f32 v176, v176, v236\n\t" \
  "v_add_f32 v177, v177, v237\n\t" \
  "v_mfma_f32_16x16x32_bf16 v[112:115], v[164:167], v[156:159], v[112:115]\n\t" \
  "v_add_f32 v178, v178, v238\n\t" \
  "v_add_f32 v179, v179, v239\n\t" \
  "s_waitcnt vmcnt(1)\n\t" \
  "v_mfma_f32_16x16x32_bf16 v[100:103], v[128:131], v[36:39], v[100:103]\n\t" \
  "s_waitcnt lgkmcnt(3)\n\t" \
  "v_add_f32 v180, v180, v240\n\t" \
  "v_add_f32 v181, v181, v241\n\t" \
  "v_mfma_f32_16x16x32_bf16 v[148:151], v[116:119], v[36:39], v[148:151]\n\t" \
  "v_add_f32 v182, v182, v242\n\t" \
  "v_add_f32 v183, v183, v243\n\t" \
  "v_mfma_f32_16x16x32_bf16 v[152:155], v[44:47], v[36:39], v[152:155]\n\t" \
  "s_waitcnt lgkmcnt(2)\n\t" \
  "v_add_f32 v184, v184, v244\n\t" \
  "v_add_f32 v185, v185, v245\n\t" \
  "v_mfma_f32_16x16x32_bf16 v[108:111], v[40:43], v[36:39], v[108:111]\n\t" \
  "v_add_f32 v186, v186, v246\n\t" \
  "v_add_f32 v187, v187, v247\n\t" \
  "v_mfma_f32_16x16x32_bf16 v[112:115], v[124:127], v[36:39], v[112:115]\n\t" \
  "s_waitcnt lgkmcnt(1)\n\t" \
  "v_add_f32 v188, v188, v248\n\t" \
  "v_add_f32 v189, v189, v249\n\t" \
  "s_waitcnt vmcnt(0)\n\t" \
  "v_mfma_f32_16x16x32_bf16 v[100:103], v[28:31], v[104:107], v[100:103]\n\t" \
  "v_add_f32 v190, v190, v250\n\t" \
  "v_add_f32 v191, v191, v251\n\t" \
  "v_mfma_f32_16x16x32_bf16 v[148:151], v[20:23], v[104:107], v[148:151]\n\t" \
  "s_waitcnt lgkmcnt(0)\n\t" \
  "v_add_f32 v192, v192, v252\n\t" \
  "v_add_f32 v193, v193, v253\n\t" \
  "v_mfma_f32_16x16x32_bf16 v[152:155], v[76:79], v[104:107], v[152:155]\n\t" \
  "v_add_f32 v194, v194, v254\n\t" \
  "v_add_f32 v195, v195, v255\n\t" \
  "v_mfma_f32_16x16x32_bf16 v[108:111], v[120:123], v[104:107], v[108:111]\n\t" \
  "v_mfma_f32_16x16x32_bf16 v[112:115], v[72:75], v[104:107], v[112:115]\n\t" \
  "s_nop 4\n\t" \
  "v_fmac_f32 v100, v176, v17\n\t" \
  "v_fmac_f32 v101, v177, v17\n\t" \
  "v_fmac_f32 v102, v178, v17\n\t" \
  "v_fmac_f32 v103, v179, v17\n\t" \
  "v_cvt_pk_bf16_f32 v100, v100, v101\n\t" \
  "v_cvt_pk_bf16_f32 v101, v102, v103\n\t" \
  "ds_write_b64 v54, v[100:101] offset:0\n\t" \
  "v_fmac_f32 v148, v180, v17\n\t" \
  "v_fmac_f32 v149, v181, v17\n\t" \
  "v_fmac_f32 v150, v182, v17\n\t" \
  "v_fmac_f32 v151, v183, v17\n\t" \
  "v_cvt_pk_bf16_f32 v148, v148, v149\n\t" \
  "v_cvt_pk_bf16_f32 v149, v150, v151\n\t" \
  "ds_write_b64 v54, v[148:149] offset:32\n\t" \
  "v_fmac_f32 v152, v184, v17\n\t" \
  "v_fmac_f32 v153, v185, v17\n\t" \
  "v_fmac_f32 v154, v186, v17\n\t" \
  "v_fmac_f32 v155, v187, v17\n\t" \
  "v_cvt_pk_bf16_f32 v152, v152, v153\n\t" \
  "v_cvt_pk_bf16_f32 v153, v154, v155\n\t" \
  "ds_write_b64 v54, v[152:153] offset:64\n\t" \
  "v_fmac_f32 v108, v188, v17\n\t" \
  "v_fmac_f32 v109, v189, v17\n\t" \
  "v_fmac_f32 v110, v190, v17\n\t" \
  "v_fmac_f32 v111, v191, v17\n\t" \
  "v_cvt_pk_bf16_f32 v108, v108, v109\n\t" \
  "v_cvt_pk_bf16_f32 v109, v110, v111\n\t" \
  "ds_write_b64 v54, v[108:109] offset:96\n\t" \
  "v_fmac_f32 v112, v192, v17\n\t" \
  "v_fmac_f32 v113, v193, v17\n\t" \
  "v_fmac_f32 v114, v194, v17\n\t" \
  "v_fmac_f32 v115, v195, v17\n\t" \
  "v_cvt_pk_bf16_f32 v112, v112, v113\n\t" \
  "v_cvt_pk_bf16_f32 v113, v114, v115\n\t" \
  "ds_write_b64 v54, v[112:113] offset:128\n\t" \
  "ds_read_b128 v[216:219], v55 offset:0\n\t" \
  "ds_read_b128 v[220:223], v56 offset:1024\n\t" \
  "ds_read_b128 v[224:227], v57 offset:2048\n\t" \
  "s_waitcnt lgkmcnt(2)\n\t" \
  "buffer_store_dwordx4 v[216:219], v58, %[ro], %[oo] offen sc1\n\t" \
  "s_waitcnt lgkmcnt(1)\n\t" \
  "buffer_store_dwordx4 v[220:223], v59, %[ro], %[oo] offen sc1\n\t" \
  "s_waitcnt lgkmcnt(0)\n\t" \
  "buffer_store_dwordx4 v[224:227], v168, %[ro], %[oo] offen sc1\n\t" \
  "s_nop 1\n\t"
#define P2_CLOBBERS "v8", "v9", "v10", "v11", "v12", "v13", "v14", "v15", "v16", "v17", "v18", "v19", "v20", "v21", "v22", "v23", "v24", "v25", "v26", "v27", "v28", "v29", "v30", "v31", "v32", "v33", "v34", "v35", "v36", "v37", "v38", "v39", "v40", "v41", "v42", "v43", "v44", "v45", "v46", "v47", "v48", "v49", "v50", "v51", "v52", "v53", "v54", "v55", "v56", "v57", "v58", "v59", "v60", "v61", "v62", "v63", "v64", "v65", "v66", "v67", "v68", "v69", "v70", "v71", "v72", "v73", "v74", "v75", "v76", "v77", "v78", "v79", "v80", "v81", "v82", "v83", "v84", "v85", "v86", "v87", "v88", "v89", "v90", "v91", "v92", "v93", "v94", "v95", "v96", "v97", "v98", "v99", "v100", "v101", "v102", "v103", "v104", "v105", "v106", "v107", "v108", "v109", "v110", "v111", "v112", "v113", "v114", "v115", "v116", "v117", "v118", "v119", "v120", "v121", "v122", "v123", "v124", "v125", "v126", "v127", "v128", "v129", "v130", "v131", "v132", "v133", "v134", "v135", "v136", "v137", "v138", "v139", "v140", "v141", "v142", "v143", "v144", "v145", "v146", "v147", "v148", "v149", "v150", "v151", "v152", "v153", "v154", "v155", "v156", "v157", "v158", "v159", "v160", "v161", "v162", "v163", "v164", "v165", "v166", "v167", "v168", "v169", "v170", "v171", "v172", "v173", "v174", "v175", "v176", "v177", "v178", "v179", "v180", "v181", "v182", "v183", "v184", "v185", "v186", "v187", "v188", "v189", "v190", "v191", "v192", "v193", "v194", "v195", "v196", "v197", "v198", "v199", "v200", "v201", "v202", "v203", "v204", "v205", "v206", "v207", "v208", "v209", "v210", "v211", "v212", "v213", "v214", "v215", "v216", "v217", "v218", "v219", "v220", "v221", "v222", "v223", "v224", "v225", "v226", "v227", "v228", "v229", "v230", "v231", "v232", "v233", "v234", "v235", "v236", "v237", "v238", "v239", "v240", "v241", "v242", "v243", "v244", "v245", "v246", "v247", "v248", "v249", "v250", "v251", "v252", "v253", "v254", "v255", "s60", "s61", "s62", "s63", "s64", "s65", "s66", "s67", "s68", "s69", "s70", "s71", "s72", "s73", "s74", "s75", "s76", "s77", "s78", "s79", "s80", "s81", "s82", "s83", "s84", "s85", "s86", "s87", "s88", "s89", "s90", "s91", "s92", "s93", "s94", "s95"

DI f32x4 mfma32(v4u a, v4u b, f32x4 c) {
  return __builtin_amdgcn_mfma_f32_16x16x32_bf16(__builtin_bit_cast(bf16x8, a), __builtin_bit_cast(bf16x8, b), c, 0, 0, 0);
}
DI unsigned pk2(f32x2 v) { return __builtin_bit_cast(unsigned, __builtin_convertvector(v, bf16x2)); }
DI v2u pk4(f32x4 v) { return v2u{pk2(v.xy), pk2(v.zw)}; }
DI v4u cat(v2u a, v2u b) { return v4u{a[0], a[1], b[0], b[1]}; }
DI float sel4(int g, float a, float b, float c, float d) {
  const float x = (g & 1) ? b : a, y = (g & 1) ? d : c;
  return (g & 2) ? y : x;
}
DI f32x4 mul4(f32x4 v, float s) {
  float a = v[0] * s, b = v[1] * s, c = v[2] * s, d = v[3] * s;
  asm("" : "+v"(a)); asm("" : "+v"(b)); asm("" : "+v"(c)); asm("" : "+v"(d));
  return f32x4{a, b, c, d};
}
DI v2u tr_read(LAS char* p) { return __builtin_bit_cast(v2u, __builtin_amdgcn_ds_read_tr16_b64_v4i16((LAS s16x4*)p)); }
DI float max_eps(float t, int eps) { return __builtin_bit_cast(float, max(__builtin_bit_cast(int, t), eps)); }
DI f32x2 phi2(f32x2 t, int eps) {
  const f32x2 u = t * 1.44269504088896f;
  const f32x2 e = {__builtin_amdgcn_fmed3f(__builtin_amdgcn_exp2f(u[0]), 0.f, 1.f), __builtin_amdgcn_fmed3f(__builtin_amdgcn_exp2f(u[1]), 0.f, 1.f)};
  const f32x2 m = {max_eps(t[0], eps), max_eps(t[1], eps)};
  return e + m;
}
DI f32x4 max4_eps(f32x4 t, int eps) {
  const float a = t[0], b = t[1], c = t[2], d = t[3];
  return f32x4{max_eps(a, eps), max_eps(b, eps), max_eps(c, eps), max_eps(d, eps)};
}
DI float exp_clamped(float u) { return __builtin_amdgcn_fmed3f(__builtin_amdgcn_exp2f(u), 0.f, 1.f); }
DI f32x4 exp4_clamped(f32x4 u) { return f32x4{exp_clamped(u[0]), exp_clamped(u[1]), exp_clamped(u[2]), exp_clamped(u[3])}; }
DI v2u phi_pk4(f32x4 t, int eps) { return v2u{pk2(phi2(t.xy, eps)), pk2(phi2(t.zw, eps))}; }
DI unsigned relu_pk(unsigned v) { return __builtin_bit_cast(unsigned, __builtin_elementwise_max(__builtin_bit_cast(s16x2, v), s16x2{0, 0})); }
DI v4u relu_frag(f32x4 a, f32x4 b) { return v4u{relu_pk(pk2(a.xy)), relu_pk(pk2(a.zw)), relu_pk(pk2(b.xy)), relu_pk(pk2(b.zw))}; }
DI v4u ld128(rsrc_t rs, int voff, int soff) { return __builtin_amdgcn_raw_buffer_load_b128(rs, voff, soff, AUX_SC1); }
DI void st128(v4u v, rsrc_t rs, int voff, int soff) { __builtin_amdgcn_raw_buffer_store_b128(v, rs, voff + soff, 0, AUX_SC1); }
DI rsrc_t mk_rsrc(const void* p, int bytes) { return __builtin_amdgcn_make_buffer_rsrc((void*)p, 0, bytes, 0x00020000); }
DI void dma16(rsrc_t rs, LAS char* dst, int voff, int soff) { __builtin_amdgcn_raw_ptr_buffer_load_lds(rs, (LAS void*)dst, 16, voff, soff, 0, 0); }
DI u64 uni64(u64 v) { return ((u64)(unsigned)__builtin_amdgcn_readfirstlane((unsigned)(v >> 32)) << 32) | (unsigned)__builtin_amdgcn_readfirstlane((unsigned)v); }
DI void warm(const GAS char* p, unsigned dump) {
  asm volatile("s_mov_b32 m0, %1\n\ts_nop 0\n\tglobal_load_lds_dword %0, off" :: "v"(p), "s"(dump) : "memory");
}

template <int SL = 1>
DI void wait_flag(rsrc_t rs, int voff, int soff, u64 tag) {
#pragma unroll 1
  for (unsigned i = 0; i < SPIN_CAP; ++i) {
    asm volatile("" ::: "memory");
    const v2u v = __builtin_amdgcn_raw_buffer_load_b64(rs, voff, soff, AUX_SC1);
    if (__all(v[0] == (unsigned)tag && v[1] == (unsigned)(tag >> 32))) break;
    __builtin_amdgcn_s_sleep(SL);
  }
  __builtin_amdgcn_fence(__ATOMIC_ACQUIRE, "wavefront");
}
DI unsigned lds_arrive(unsigned addr) {
  unsigned old;
  asm volatile("s_mov_b64 exec, 1\n\tds_add_rtn_u32 %0, %1, %2\n\ts_mov_b64 exec, -1\n\ts_waitcnt lgkmcnt(0)" : "=&v"(old) : "v"(addr), "v"(1u) : "memory");
  return __builtin_amdgcn_readfirstlane(old);
}
DI void lds_sync(unsigned addr, unsigned n) {
  unsigned v = lds_arrive(addr) + 1;
#pragma unroll 1
  for (unsigned i = 0; v < n && i < (1u << 22); ++i) {
    unsigned x;
    asm volatile("ds_read_b32 %0, %1\n\ts_waitcnt lgkmcnt(0)" : "=v"(x) : "v"(addr) : "memory");
    v = __builtin_amdgcn_readfirstlane(x);
  }
}
DI void publish(unsigned addr, unsigned n, rsrc_t rs, int flag, u64 tag) {
  WAIT_VM(0);
  if (lds_arrive(addr) == n - 1)
    asm volatile("s_mov_b64 exec, 1\n\tbuffer_store_dwordx2 %0, off, %1, %2 sc1\n\ts_mov_b64 exec, -1" :: "v"(tag), "s"(rs), "s"(flag) : "memory");
}

struct Frags { v4u a[4]; v4u b[7]; };
template <int V> constexpr int brow(int n) { return V == 0 ? (n < 5 ? 320 : 128) : (n < 4 ? 256 : 192); }
DI LAS char* lp(unsigned a) { return (LAS char*)(size_t)a; }
template <int V>
DI void read_frags(Frags& f, int ks, unsigned aad, const unsigned (&bad)[7], unsigned gad, unsigned lo) {
#pragma unroll
  for (int i = 0; i < 7; ++i) {
    const int n = V == 0 ? i : (i + 6) % 7;
    LAS char* p = lp(bad[n]) + ks * 32 * brow<V>(n);
    f.b[n] = cat(tr_read(p), tr_read(p + 4 * brow<V>(n)));
    if (V == 1 && n == 6) {
      const v4u gw = *(LAS v4u*)(lp(gad) + ks * 64);
      #pragma unroll
      for (int j = 0; j < 4; ++j) f.b[n][j] = __builtin_amdgcn_perm(f.b[n][j], gw[j], lo);
    }
    if (i < 4) f.a[i] = *(LAS v4u*)(lp(aad) + i * 2048);
  }
}
template <int V, int N0 = 0, int N1 = 7>
DI void mma_block(f32x4 (&acc)[4][7], const Frags& f) {
#pragma unroll
  for (int n = N0; n < N1; ++n)
#pragma unroll
    for (int i = 0; i < 4; ++i) {
      const int m = (SNAKE && (n & 1)) ? 3 - i : i;
      acc[m][n] = (V == 0 && n < 5) ? mfma32(f.a[m], f.b[n], acc[m][n]) : mfma32(f.b[n], f.a[m], acc[m][n]);
    }
}
template <int NR, int NV = 0, int NV2 = 1>
DI void interleave() {
#pragma unroll
  for (int i = 0; i < NR; ++i) {
    __builtin_amdgcn_sched_group_barrier(0x008, 1, 0);
    __builtin_amdgcn_sched_group_barrier(0x080, 1, 0);
  }
#pragma unroll
  for (int i = 0; i < NV; ++i) {
    __builtin_amdgcn_sched_group_barrier(0x008, 1, 0);
    __builtin_amdgcn_sched_group_barrier(0x002, NV2, 0);
  }
  __builtin_amdgcn_sched_group_barrier(0x008, 28 - NR - NV, 0);
}

template <int V, class Pre, class Post, class Fill, class Mid>
DI void p1_loop(f32x4 (&acc)[4][7], LAS char* L, int lane, int wave, int wm, int wn, int hg, GAS char* ws, Pre& pre, Post& post, Fill& fill, Mid& mid) {
  const int g = lane >> 4, q = (lane >> 2) & 3, p = lane & 3, ph = p >> 1;
  unsigned a0 = (unsigned)(size_t)L + (64 * wm + (lane & 15)) * 128 + 16 * (g ^ (lane & 7)), a1 = a0 ^ 64;
  PIN(a0); PIN(a1);
  unsigned bad[7];
#pragma unroll
  for (int n = 0; n < 7; ++n) {
    const int c = 2 * n + ph;
    int o;
    if (V == 0 && n < 5) o = (8 * g + q) * 320 + 16 * ((c < 5 ? 5 * wn + c : 10 + ((5 * wn + c - 5) ^ 1)) ^ (2 * (g & 1)));
    else if (V == 0) o = 20480 + (8 * g + q) * 128 + 16 * ((4 * wn + c - 10) ^ (2 * ((q >> 1) | ((g & 1) << 1))));
    else if (n < 4) o = 12288 + (8 * g + q) * 256 + 16 * ((8 * wn + c) ^ (2 * (q | ((g & 1) << 2))));
    else o = (8 * g + q) * 192 + 16 * ((5 * wn + (c == 13 ? 3 : c - 8) + 2 * (hg & 1)) ^ (2 * (g & 1)));
    bad[n] = (unsigned)(size_t)L + BOFF + o + 8 * (p & 1);
    PIN(bad[n]);
  }
  unsigned gad = (unsigned)(size_t)L + L_GATE + 16 * g;
  PIN(gad);
  unsigned lo = (lane & 15) < 8 ? 0x07060504u : 0x03020100u;
  PIN(lo);
  v4u A[4][2], gw[2];
  v2u B[7][4];
  auto rdA = [&](int m, int k) { A[m][k] = *(LAS v4u*)(lp(k ? a1 : a0) + m * 2048); };
  auto rdB = [&](int n, int i) { B[n][i] = tr_read(lp(bad[n]) + ((i >> 1) * 32 + (i & 1) * 4) * brow<V>(n)); };
  auto mm = [&](int m, int n, int k) {
    const v4u bf = cat(B[n][2 * k], B[n][2 * k + 1]);
    if (V == 0 && n < 5) asm volatile("v_mfma_f32_16x16x32_bf16 %0, %1, %2, %0" : "+v"(acc[m][n]) : "v"(A[m][k]), "v"(bf));
    else asm volatile("v_mfma_f32_16x16x32_bf16 %0, %1, %2, %0" : "+v"(acc[m][n]) : "v"(bf), "v"(A[m][k]));
    FENCE();
  };
  int dl = STAGE;
  auto head = [&](bool last) {
#pragma unroll
    for (int j = 0; j < 16; ++j) {
      mm(j >> 2, (j >> 1) & 1, j & 1);
      if (j < 2) rdA(3, j); else rdB(2 + ((j - 2) >> 2), (j - 2) & 3);
      FENCE();
    }
#pragma unroll
    for (int j = 0; j < 24; ++j) {
      const int n = 2 + (j >> 3), i = (j >> 1) & 3;
      mm((n & 1) ? i : 3 - i, n, j & 1);
      if (j < 6) rdB(2 + ((j + 14) >> 2), (j + 14) & 3);
      else if (V == 1 && j < 8) gw[j - 6] = *(LAS v4u*)(lp(gad) + (j - 6) * 64);
      else if (!last && j >= 8 && j < 13) {
        if (j == 8) { a0 += dl; a1 += dl; PIN(a0); PIN(a1); }
        else { bad[2 * j - 18] += dl; PIN(bad[2 * j - 18]); if (j < 12) { bad[2 * j - 17] += dl; PIN(bad[2 * j - 17]); } else if (V == 1) { gad += 128; PIN(gad); } }
      } else if (V == 1 && j >= 16 && j < 20) {
        const int i2 = j - 16;
        B[6][i2][0] = __builtin_amdgcn_perm(B[6][i2][0], gw[i2 >> 1][2 * (i2 & 1)], lo);
        B[6][i2][1] = __builtin_amdgcn_perm(B[6][i2][1], gw[i2 >> 1][2 * (i2 & 1) + 1], lo);
        PIN(B[6][i2]);
      }
      if (last) {
        fill(16 + j);
        if (V == 0 && j == AB10) asm volatile("s_waitcnt lgkmcnt(0)\n\ts_barrier" ::: "memory");
      }
      FENCE();
    }
  };
#pragma unroll
  for (int m = 0; m < 4; ++m)
#pragma unroll
    for (int n = 0; n < 7; ++n) { acc[m][n] = f32x4{0.f, 0.f, 0.f, 0.f}; PIN(acc[m][n]); }
  FENCE();
  pre();
  WG_BARRIER();
#pragma unroll
  for (int i = 0; i < 2; ++i) { rdB(0, 2 * i); rdB(0, 2 * i + 1); rdA(0, i); }
#pragma unroll
  for (int i = 0; i < 4; ++i) { rdB(1, i); rdA(1 + (i >> 1), i & 1); }
  FENCE();
  int ph3 = 0;
#pragma unroll 1
  for (int t = 0; t < 9; ++t) {
    dl = ph3 == 2 ? -2 * STAGE : STAGE;
    ph3 = ph3 == 2 ? 0 : ph3 + 1;
    head(false);
    if (V == 1) asm volatile("s_cmp_lg_u32 %0, %1\n\ts_cbranch_scc1 PD_%=\n\ts_waitcnt vmcnt(0)\nPD_%=:" :: "s"(t), "i"(PT_DRAIN + 1) : "memory", "scc");
    WG_BARRIER();
#pragma unroll
    for (int j = 0; j < 16; ++j) {
      mm(j >> 2, 5 + ((j >> 1) & 1), j & 1);
      if (j < 4) rdB(0, j); else if (j < 6) rdA(0, j - 4); else if (j < 10) rdB(1, j - 6); else if (j < 14) rdA(1 + ((j - 10) >> 1), j & 1);
      FENCE();
    }
  }
  head(true);
  post();
  mid();
#pragma unroll
  for (int j = 0; j < 16; ++j) {
    mm(j >> 2, 5 + ((j >> 1) & 1), j & 1);
    fill(40 + j);
    FENCE();
  }
  asm volatile("s_nop 7");
  FENCE();
}

struct Args {
  const char *x, *wqkv, *w1, *ws, *w2, *wo, *gw, *out;
};

typedef unsigned v16u __attribute__((ext_vector_type(16)));
__global__ __launch_bounds__(512, 2) void fused(const Args) {
  extern __shared__ __attribute__((aligned(16))) char smem[];
  LAS char* L = (LAS char*)smem;
  const unsigned dump = (unsigned)(size_t)L + L_DUMP;
  v16u ka;
  u64 pc;
  {
    unsigned t, u;
    asm volatile("s_load_dwordx16 %0, %4, 0x0\n\ts_getpc_b64 %1\n\ts_mov_b32 m0, %7\n\ts_lshl_b32 %3, %5, %9\n\ts_and_b32 %3, %3, %10\n\t"
                 "v_lshlrev_b32 %2, %11, %6\n\tv_and_b32 %2, 0x1f80, %2\n\tv_or_b32 %2, %3, %2\n\tv_min_u32 %2, %8, %2\n\tglobal_load_lds_dword %2, %1"
                 : "=&s"(ka), "=&s"(pc), "=&v"(t), "=&s"(u)
                 : "s"(__builtin_amdgcn_kernarg_segment_ptr()), "s"(blockIdx.x), "v"(threadIdx.x), "s"(dump), "i"(CODE_BYTES - 256),
                   "i"(10 - CSH), "i"(0x8000 - (0x2000 >> CSH)), "i"(4 - CSH) : "memory", "scc");
  }
  const int tid = threadIdx.x, lane = tid & 63, wave = __builtin_amdgcn_readfirstlane(tid >> 6);
  const int b = blockIdx.x, xcd = b & 7, bi = b >> 3;
  const int r = 8 * (xcd & 1) + (bi & 7), typeB = bi >> 4, hg = 2 * (xcd >> 1) + ((bi >> 3) & 1);
  const int rt = 8 * (xcd >> 1) + 2 * ((bi >> 2) & 3) + ((typeB + bi) & 1), ct = 4 * (xcd & 1) + (bi & 3), r2 = rt >> 1, rot = 2 * (((bi >> 2) + HROT * (bi & 3)) & 3);
  LAS unsigned* W = (LAS unsigned*)(L + L_W);
  STAMP_DECL
  STAMP(0);
  LITE0();
  Args a;
  {
    const int id = b & 127, k = wave < 2 ? 0 : (wave < 5 ? 1 : 2);
    const int v7 = (lane & 7) * (k == 0 ? 10 : 0), vs = lane >> (k == 0 ? 3 : 0);
    auto line = [&](int s0, int ns) {
      const int per = (k == 0 ? 1 : 6 - k) * ns;
      const int c = k == 0 ? (16 * id + 8 * wave) * 10 + s0 : 384 * (6 - k) * s0 + (3 * id + wave + 1 - 3 * k) * per;
      return (unsigned)(c + v7 + min(vs, per - 1)) * 128u;
    };
    unsigned o1 = line(typeB ? GA : 0, typeB ? GB : GA), o2 = line(GA + GB, 10 - GA - GB);
    asm volatile("s_waitcnt lgkmcnt(0)" : "+s"(ka), "+v"(o1), "+v"(o2) :: "memory");
    a = __builtin_bit_cast(Args, ka);
    const char* base = k == 0 ? a.x : (k == 1 ? a.wqkv : a.w1);
    asm volatile("global_load_lds_dword %0, %2\n\ts_cmp_eq_u32 %3, 0\n\ts_cbranch_scc1 2\n\tglobal_load_lds_dword %1, %2" :: "v"(o1), "v"(o2), "s"(base), "s"(typeB) : "memory", "scc");
    if (wave == TLBW - 1) {
      const int j = (bi & 7) * 8 + (lane & 7);
      const char* tb = j < 30 ? a.w2 : (j < 43 ? a.wo : (j < 44 ? a.gw : (j < 54 ? a.ws : a.out)));
      const int jo = j < 30 ? j * 65536 : (j < 43 ? (j - 30) * 65536 : (j < 44 ? 0 : (j < 50 ? OFF_A2 + (j == 49 ? 32 : 7 * (j - 44)) * A2_STRIDE - 128
                   : (j < 54 ? (j == 53 ? OFF_PAD : OFF_B2 + 4 * (j - 50) * B2_STRIDE - 128) : (j - 54) * 262144))));
      asm volatile("global_load_lds_dword %0, off" :: "v"((const GAS char*)tb + jo) : "memory");
    }
    FENCE();
  }
  GAS char* ws = (GAS char*)a.ws;
  if (__builtin_expect(wave >= 4, 1)) {
    const int w = wave - 4;
    const rsrc_t rx = mk_rsrc(a.x, S * D * 2), rq = mk_rsrc(a.wqkv, D * 3 * D * 2), r1 = mk_rsrc(a.w1, D * FF * 2);
    const int va = (lane >> 3) * (D * 2) + 16 * ((lane ^ (lane >> 3)) & 7);
    int sx = (128 * r + 32 * w) * (D * 2);
    auto issueA = [&](int so, int j0, int j1) {
#pragma unroll
      for (int j = j0; j < j1; ++j) dma16(rx, L + so + (4 * w + j) * 1024, va, sx + j * 8 * (D * 2));
    };
    if (XFIRST && !typeB) issueA(0, 0, 4);
    FENCE();
    STAMP(1);
    int vb[7];
    if (!typeB) {
#pragma unroll
      for (int j = 0; j < 5; ++j) {
        const int i = 64 * j + lane, rl = (i * 3277) >> 16, pos = (i - 20 * rl) ^ ((rl >> 2) & 2);
        vb[j] = rl * (3 * D * 2) + (pos < 10 ? 16 * pos : 1280 - 160 + 16 * (pos ^ 1));
      }
#pragma unroll
      for (int j = 0; j < 2; ++j) vb[5 + j] = (8 * j + (lane >> 3)) * (FF * 2) + 16 * ((lane & 7) ^ (((lane >> 3) & 2) | (4 * j)));
    } else {
#pragma unroll
      for (int j = 0; j < 3; ++j) {
        const int i = 64 * j + lane, rl = (i * 5462) >> 16, pos = (i - 12 * rl) ^ ((rl >> 2) & 2);
        vb[j] = rl * (3 * D * 2) + 16 * pos;
      }
#pragma unroll
      for (int j = 0; j < 4; ++j) vb[3 + j] = (4 * j + (lane >> 4)) * (FF * 2) + 16 * ((lane & 15) ^ (2 * ((lane >> 4) | ((j >> 1) << 2))));
    }
    const rsrc_t rm = typeB ? r1 : rq;
    int sq = 16 * w * (3 * D * 2) + (typeB ? (160 * hg) & ~63 : 1280 + 160 * hg);
    int s1 = 16 * w * (FF * 2) + (typeB ? 1024 + 256 * hg : 128 * hg);
    const int lq = BOFF + w * (typeB ? 3072 : 5120), l1 = BOFF + (typeB ? 12288 + w * 4096 - 3072 : 20480 + w * 2048 - 5120);
    auto issueB = [&](int so, int j0, int j1) {
      const int sm = typeB ? s1 : sq, lm = typeB ? l1 : lq;
#pragma unroll
      for (int j = j0; j < j1; ++j)
        dma16(j < 3 ? rq : (j < 5 ? rm : r1), L + so + (j < 3 ? lq : (j < 5 ? lm : l1)) + j * 1024, vb[j], j < 3 ? sq : (j < 5 ? sm : s1));
    };
    const rsrc_t rs = mk_rsrc(a.ws, WS_BYTES);
    const int par = ((bi >> 3) + bi) & 1;
    u64 tag = 0, fl = 0;
    const int cl0 = (5 * ct) >> 2, vf0 = (min(13 * cl0 + min(lane & 31, 25), 127) + (PT_SPLIT ? 128 * (lane >> 5) : 128)) * 128;
    int so = 0;
#pragma unroll 1
    for (int t = 0; t <= 9; ++t) {
      if (t == F0_AT && w == 0) asm volatile("buffer_load_dwordx2 %0, %1, %2, %3 offen sc1" : "+v"(fl) : "v"(vf0), "s"(rs), "s"(OFF_F0) : "memory");
      const bool nox = XFIRST && !typeB && t == 0;
      if (par == 0) { if (!nox) issueA(so, 0, 4); issueB(so, 0, 4); } else { issueB(so, 2, 7); if (!nox) issueA(so, 0, 3); }
      if (t >= 1) {
        WAIT_VM(8);
        if (t == 9 && w == 0) {
          asm volatile("" : "+v"(fl));
#pragma unroll 1
          for (unsigned i = 0; i < SPIN_CAP && !__all(fl == tag); ++i)
            asm volatile("s_sleep 2\n\tbuffer_load_dwordx2 %0, %1, %2, %3 offen sc1\n\ts_waitcnt vmcnt(0)" : "+v"(fl) : "v"(vf0), "s"(rs), "s"(OFF_F0) : "memory");
        }
        asm volatile("s_barrier" ::: "memory");
      }
      if (par == 0) issueB(so, 4, 7); else { if (!nox) issueA(so, 3, 4); issueB(so, 0, 2); }
      sx += 128;
      so += STAGE; if (so == 3 * STAGE) so = 0;
      sq += 64 * 3 * D * 2; s1 += 64 * FF * 2;
      if (t == PT_DRAIN + 3) {
        u64 tv;
        asm volatile("ds_read_b64 %0, %1 offset:32\n\ts_waitcnt lgkmcnt(0)" : "=&v"(tv) : "v"((unsigned)(size_t)L + L_W) : "memory");
        tag = uni64(tv);
        if ((typeB || PT_SPLIT) && tid == 256) __hip_atomic_store((GAS u64*)(ws + OFF_F0 + (typeB * 128 + r * 8 + hg) * 128), tag, RLX_AGENT);
      }
    }
    int vo = lane * 16;
    PIN(vo);
    {
      const u64 wsa = (u64)a.ws;
      const v4u srd = {(unsigned)wsa, (unsigned)(wsa >> 32) & 0xffffu, (unsigned)WS_BYTES, 0x00020000u};
      const int abase = OFF_A2 + rt * A2_STRIDE, bbase = OFF_B2 + ct * B2_STRIDE;
      const int kB = 16 + 8 * w, kA = 4 * w;
      const u64 oa = (u64)a.out;
      const v4u sro = {(unsigned)oa, (unsigned)(oa >> 32) & 0xffffu, (unsigned)(S * D * 2), 0x00020000u};
      const int r4 = ((rot >> 1) + OROT * (bi & 3)) & 3;
      unsigned ta, tb, cnt, rdy;
      asm volatile(P2_ASM
                   : [ta] "=&s"(ta), [tb] "=&s"(tb), [cnt] "=&s"(cnt), [rdy] "=&s"(rdy)
                   : [vo] "v"(vo), [rs] "s"(srd), [ro] "s"(sro), [oo] "s"((64 * rt + 16 * w) * (D * 2) + ct * 160), [tag] "s"(tag),
                     [l0] "s"((unsigned)(size_t)L), [w] "s"(w), [rot] "s"(rot), [r4] "s"(r4), [r20] "s"(5 * r4),
                     [aB] "s"(abase + kB * 4096), [aA] "s"(abase + kA * 4096), [aO] "s"(abase + 48 * 4096),
                     [bB] "s"(bbase + kB * 5120), [bA] "s"(bbase + kA * 5120), [bO] "s"(bbase + 48 * 5120),
                     [f1B] "s"(OFF_F1 + (r2 * 16 + 8 + 2 * w) * 128), [go] "s"(OFF_G + ((r2 * 8 + 2 * w) * 128 + 64 * (rt & 1) + 16 * w) * 4)
                   : "memory", "vcc", "scc", P2_CLOBBERS);
    }
    const int lane2 = vo >> 4;
    LITE1(lane2);
    STAMP(9);
    STAMP_OUT();
    return;
  }

  const rsrc_t rs = mk_rsrc(a.ws, WS_BYTES);
  const int wm = wave >> 1, wn = wave & 1;
  const int g = lane >> 4, tk = lane & 15;
  u64 tag;
  const int idx = r * 8 + hg, cl = idx < 104 ? (idx * 5042) >> 16 : (idx < 116 ? 8 : 9), nw = cl < 8 ? 13 : 12;
  const int ks0 = idx - 13 * cl + (cl == 9 ? 1 : 0) + nw * (wave + (PT_SPLIT && !typeB ? 4 : 0));
  const int njob = PT_SPLIT ? (ks0 < 68 ? 1 : 0) : (!typeB ? 0 : (ks0 + 4 * nw < 68 ? 2 : 1));
  if (wave == 3) {
    const int k = lane & 7, hi = k & 1;
    const GAS char* fl = k < 2 ? ws + (hi ? OFF_F2 : OFF_F1) + (r * 16 + typeB * 8 + hg) * 128 : ws + OFF_F0 + (typeB * 128 + idx) * 128;
    warm(fl, dump);
    W[lane] = 0u;
  }
  const u64 e = __hip_atomic_load((GAS u64*)(ws + OFF_E), RLX_AGENT);
#pragma unroll 1
  for (int u = 0; u < njob; ++u) {
    const int ks = ks0 + 4 * nw * u;
    const rsrc_t rw = ks < 48 ? mk_rsrc(a.w2, FF * D * 2) : mk_rsrc(a.wo, D * D * 2);
    const int w5 = ((ks - 48) * 205) >> 10, i5 = ks - 48 - 5 * w5;
    const bool rest = ks >= 48 && i5 == 4;
    const int row0 = ks < 48 ? 32 * ks : 160 * w5 + (rest ? 32 : 40 * i5);
    const int vw = (rest ? 40 * (lane >> 5) + ((lane >> 3) & 3) : lane >> 3) * (D * 2) + (lane & 7) * 16;
#pragma unroll
    for (int j = 0; j < 4; ++j)
      dma16(rw, L + L_SCR + (wave + 4 * u) * 4096 + j * 1024, vw, (row0 + (rest ? 4 * (j >> 1) + 80 * (j & 1) : 8 * j)) * (D * 2) + cl * 128);
  }
  if (typeB && wave == 3) {
    const rsrc_t rg = mk_rsrc(a.gw, D * 2);
    dma16(rg, L + L_GATE, lane * 16, 0);
    if (lane < 16) dma16(rg, L + L_GATE + 1024, lane * 16, 1024);
  }
  FENCE();
  STAMP(9);
  auto pre = [&]() {
    int vs[4];
    unsigned pp = (unsigned)(size_t)L + L_SCR + wave * 4096 + (lane >> 2) * 128 + (lane & 3) * 8;
    if (typeB) {
#pragma unroll
      for (int n = 0; n < 4; ++n) {
        const int nf = cl * 4 + n, c5 = (nf * 205) >> 10;
        vs[n] = OFF_B2 + c5 * B2_STRIDE + (ks0 * 5 + nf - 5 * c5) * 1024 + lane * 16;
        PIN(vs[n]);
      }
      PIN(pp);
    }
    FENCE();
    __builtin_amdgcn_s_waitcnt(0);
    if (PTSLEEP && typeB) __builtin_amdgcn_s_sleep(PTSLEEP);
#pragma unroll 1
    for (int u = 0; u < njob; ++u) {
#pragma unroll
      for (int n = 0; n < 4; ++n) {
        st128(cat(tr_read(lp(pp) + n * 32), tr_read(lp(pp) + n * 32 + 2048)), rs, vs[n], 0);
        vs[n] += nw * 20480;
      }
      pp += 16384;
    }
    tag = uni64(e) + EPOCH_STEP;
    if (wave == 3 && lane == 0) *(LAS u64*)(L + L_W + 32) = tag;
    STAMP(1);
  };
  auto post = [&]() { STAMP(2); __builtin_amdgcn_s_setprio(PRIO); FENCE(); };
  int eps = 0x358637bd;
  asm volatile("" : "+s"(eps));
  f32x4 acc[4][7];
  const int a2b = OFF_A2 + (2 * r + wm) * A2_STRIDE;
  const int myf1 = OFF_F1 + (r * 16 + typeB * 8 + hg) * 128;
  const unsigned lw = (unsigned)(size_t)L + L_W;
  if (!typeB) {
    constexpr int TD[8] = {0, 0, 1, 1, 1, 2, 2, 2}, TE[8] = {2, 3, 2, 3, 4, 2, 3, 4};
    unsigned ca[8];
#pragma unroll
    for (int i = 0; i < 8; ++i) {
      const int dl = 16 * TD[i] + 4 * g, hd = dl >= 20 ? 1 : 0;
      const int e = 16 * TE[i] + tk - 40 - 20 * hd;
      const bool isz = TE[i] == 2 && tk == 0;
      const bool ok = dl < 40 && (isz || (e >= 0 && e < 20));
      ca[i] = (unsigned)(size_t)L + (ok ? L_TAB + wm * 6720 + (((2 * wn + hd) * 21 + (isz ? 20 : e)) * 20 + dl - 20 * hd) * 4 : L_DUMP + 16 * (lane & 7));
      PIN(ca[i]);
    }
    float mxs[48];
    auto fillA = [&](int G) {
      if (G < 40) {
#pragma unroll
        for (int i = 2 * (G - 16); i < 2 * (G - 15); ++i) {
          const float t = acc[(i >> 2) & 3][i >> 4][i & 3];
          mxs[i] = max_eps(t, eps);
          PIN(mxs[i]);
        }
      }
    };
    auto midA = [&]() {
    asm volatile("s_nop 7");
    FENCE();
    f32x4 mx[4][3];
#pragma unroll
    for (int f = 0; f < 3; ++f)
#pragma unroll
      for (int m = 0; m < 4; ++m) mx[m][f] = f32x4{mxs[16 * f + 4 * m], mxs[16 * f + 4 * m + 1], mxs[16 * f + 4 * m + 2], mxs[16 * f + 4 * m + 3]};
    v4u PK[3][2], VV[3][2];
    auto phi_frag = [&](int f) {
      f32x4 ex[4];
#pragma unroll
      for (int m = 0; m < 4; ++m) ex[m] = acc[m][f] * 1.44269504088896f;
      FENCE();
#pragma unroll
      for (int m = 0; m < 4; ++m) ex[m] = exp4_clamped(ex[m]);
      FENCE();
#pragma unroll
      for (int m = 0; m < 4; ++m) ex[m] += mx[m][f];
      FENCE();
#pragma unroll
      for (int h = 0; h < 2; ++h) PK[f][h] = cat(pk4(ex[2 * h]), pk4(ex[2 * h + 1]));
    };
    f32x4 c[8];
    auto tiles = [&](int i0, int i1) {
#pragma unroll
      for (int h = 0; h < 2; ++h)
#pragma unroll
        for (int i = i0; i < i1; ++i) c[i] = mfma32(PK[TD[i]][h], VV[TE[i] - 2][h], h == 0 ? f32x4{0.f, 0.f, 0.f, 0.f} : c[i]);
    };
    phi_frag(0);
    phi_frag(1);
#pragma unroll
    for (int f = 0; f < 3; ++f)
#pragma unroll
      for (int h = 0; h < 2; ++h) {
        VV[f][h] = cat(pk4(acc[2 * h][f + 2]), pk4(acc[2 * h + 1][f + 2]));
        if (f == 0 && tk < 8) VV[f][h] = v4u{0x3F803F80u, 0x3F803F80u, 0x3F803F80u, 0x3F803F80u};
      }
    FENCE();
    {
      f32x4 ex[4];
#pragma unroll
      for (int m = 0; m < 4; ++m) ex[m] = acc[m][2] * 1.44269504088896f;
      FENCE();
      tiles(0, 5);
#pragma unroll
      for (int m = 0; m < 4; ++m) ex[m] = exp4_clamped(ex[m]);
#pragma unroll
      for (int i = 0; i < 10; ++i) { __builtin_amdgcn_sched_group_barrier(0x008, 1, 0); __builtin_amdgcn_sched_group_barrier(0x400, 1, 0); }
      FENCE();
#pragma unroll
      for (int i = 0; i < 5; ++i) *(LAS f32x4*)lp(ca[i]) = c[i];
#pragma unroll
      for (int m = 0; m < 4; ++m) ex[m] += mx[m][2];
      FENCE();
#pragma unroll
      for (int h = 0; h < 2; ++h) PK[2][h] = cat(pk4(ex[2 * h]), pk4(ex[2 * h + 1]));
    }
    FENCE();
    tiles(5, 8);
#pragma unroll
    for (int i = 5; i < 8; ++i) *(LAS f32x4*)lp(ca[i]) = c[i];
    FENCE();
    STAMP(6);
    lds_sync(lw, 4);
    {
      LAS char* p = L + L_TAB + min(tid, 209) * 32;
      const f32x4 s = *(LAS f32x4*)p + *(LAS f32x4*)(p + 6720), s2 = *(LAS f32x4*)(p + 16) + *(LAS f32x4*)(p + 6736);
      st128(cat(pk4(s), pk4(s2)), rs, tid < 210 ? tid * 16 : WS_BYTES, OFF_KVP + (hg * 16 + r) * KVP_STRIDE);
    }
    FENCE();
    };
    p1_loop<0>(acc, L, lane, wave, wm, wn, hg, ws, pre, post, fillA, midA);
    v4u hf[4];
#pragma unroll
    for (int m = 0; m < 4; ++m) { hf[m] = relu_frag(acc[m][5], acc[m][6]); PIN(hf[m]); }
    STAMP(3);
    publish(lw + 4, 4, rs, myf1, tag);
    STAMP(4);
#pragma unroll
    for (int m = 0; m < 4; ++m) st128(hf[m], rs, lane * 16 + m * 1024, a2b + (2 * hg + wn) * 4096);
    publish(lw + 12, 4, rs, OFF_F2 + (r * 16 + hg) * 128, tag);
    STAMP(5);
    if (b == 0 && wave == 0) {
      __builtin_amdgcn_s_sleep(EDELAY);
#pragma unroll 1
      for (int i = 0; i < 4; ++i) wait_flag(rs, lane * 128, OFF_F1 + i * 8192, tag);
      if (lane == 0) __hip_atomic_store((GAS u64*)(ws + OFF_E), tag, RLX_AGENT);
    }
    STAMP_OUT();
    return;
  }
  int vh[2] = {a2b + (16 + 4 * hg + 2 * wn) * 4096 + lane * 16, a2b + (17 + 4 * hg + 2 * wn) * 4096 + lane * 16};
  PIN(vh[0]); PIN(vh[1]);
  v4u hv;
  auto fillB = [&](int G) {
    const int s = G < 32 ? G - 16 : G - 20;
    if ((G < 32 || G >= 36) && s <= 32) {
      if (s > 0 && (s & 3) == 0 && G != 36) st128(hv, rs, vh[(s - 1) >> 4], (((s - 1) >> 2) & 3) * 1024);
      if (s < 32) {
        const int u = s >> 4, m = (s >> 2) & 3, j = s & 3;
        const f32x4 t = acc[m][2 * u + (j >> 1)];
        unsigned x = relu_pk(pk2((j & 1) ? t.zw : t.xy));
        PIN(x);
        hv[j] = x;
      }
    }
    if (G == 32) st128(hv, rs, vh[0], 3 * 1024);
  };
  auto midB = [&]() {};
  p1_loop<1>(acc, L, lane, wave, wm, wn, hg, ws, pre, post, fillB, midB);
  STAMP(3);
  if (LATE_B10 != 2) WG_BARRIER(); else FENCE();
  float gv[4];
#pragma unroll
  for (int m = 0; m < 4; ++m) {
    const float l0 = acc[m][6][0];
    const unsigned lg = __builtin_bit_cast(unsigned, l0);
    const unsigned up = __builtin_amdgcn_permlane32_swap(lg, lg, false, false)[1];
    const float x = __builtin_bit_cast(float, up);
    gv[m] = __builtin_amdgcn_rcpf(1.f + __expf(-x));
  }
  const float gsel = sel4(g, gv[0], gv[1], gv[2], gv[3]);
  __builtin_amdgcn_raw_buffer_store_b32(__builtin_bit_cast(unsigned, gsel), rs, wn == 0 ? lane * 4 : WS_BYTES, OFF_G + ((r * 8 + hg) * 128 + 64 * wm) * 4, AUX_SC1);
  FENCE();
  v2u pq[4][3];
#pragma unroll
  for (int m = 0; m < SHADOW; ++m)
#pragma unroll
    for (int f = 0; f < 3; ++f) pq[m][f] = phi_pk4(acc[m][4 + f], eps);
#pragma unroll
  for (int m = 0; m < SHADOW; ++m) { PIN(pq[m][0]); PIN(pq[m][1]); PIN(pq[m][2]); }
  FENCE();
  if (LATE_B10 == 2) {
    asm volatile("s_waitcnt vmcnt(0)\n\ts_barrier" ::: "memory");
    if (wave == 0) asm volatile("s_mov_b64 exec, 1\n\tbuffer_store_dwordx2 %0, off, %1, %2 sc1\n\ts_mov_b64 exec, -1" :: "v"(tag), "s"(rs), "s"(myf1) : "memory");
  } else publish(lw + 4, 4, rs, myf1, tag);
  STAMP(1);
  FENCE();
#pragma unroll
  for (int m = SHADOW; m < 4; ++m)
#pragma unroll
    for (int f = 0; f < 3; ++f) pq[m][f] = phi_pk4(acc[m][4 + f], eps);
  v4u q01[4], q12[4];
#pragma unroll
  for (int m = 0; m < 4; ++m) { q01[m] = cat(pq[m][0], pq[m][1]); q12[m] = cat(pq[m][1], pq[m][2]); PIN(q01[m]); PIN(q12[m]); }
  unsigned kd[10], ra[4][4];
  {
    const unsigned zero = lw + 40, tb = (unsigned)(size_t)L + L_TAB + wn * 1680;
    const unsigned h0u0 = tb + 8 * g, h0u1 = tb + 32, h1u1 = tb + 840 + 8 * g - 8, h1u2 = tb + 840 + 24 + 8 * g;
    const bool g0 = g == 0, g01 = g < 2, e0 = tk < 4;
    kd[0] = h0u0 + 40 * tk;
    kd[1] = g0 ? h0u1 + 40 * tk : zero;
    kd[2] = e0 ? h0u0 + 40 * (16 + tk) : zero;
    kd[3] = e0 ? (g0 ? h0u1 + 40 * (16 + tk) : zero) : (g0 ? zero : h1u1 + 40 * (tk - 4));
    kd[4] = (!e0 && g01) ? h1u2 + 40 * (tk - 4) : zero;
    kd[5] = h0u0 + 800;
    kd[6] = g0 ? h0u1 + 800 : zero;
    kd[7] = g0 ? zero : h1u1 + 800;
    kd[8] = g01 ? h1u2 + 800 : zero;
    kd[9] = zero;
#pragma unroll
    for (int i = 0; i < 10; ++i) PIN(kd[i]);
#pragma unroll
    for (int m = 0; m < 4; ++m) {
      const bool me = (tk >> 2) == m;
      ra[m][0] = (me && !g0) ? h1u1 + 40 * (12 + (tk & 3)) : zero;
      ra[m][1] = (me && g01) ? h1u2 + 40 * (12 + (tk & 3)) : zero;
      ra[m][2] = ra[m][0] + 160; ra[m][3] = ra[m][1] + 160;
      PIN(ra[m][0]); PIN(ra[m][1]); PIN(ra[m][2]); PIN(ra[m][3]);
    }
  }
#pragma unroll
  for (int m = 0; m < 4; ++m) PIN(gv[m]);
  v4u ia, ib;
#pragma unroll
  for (int j = 0; j < 4; ++j) {
    const unsigned one = (tk >> 1 & 3) == j ? (tk & 1 ? 0x3F800000u : 0x3F80u) : 0u;
    ia[j] = g == (tk >> 3) ? one : 0u;
    ib[j] = g == 2 + (tk >> 3) ? one : 0u;
  }
  unsigned ta = (unsigned)(size_t)L + L_TAB + wave * 1024 + (g >> 1) * 256 + tk * 16 + (g & 1) * 8;
  f32x4 e4 = {1e-6f, 1e-6f, 1e-6f, 1e-6f};
  int vmain = a2b + (48 + 5 * (hg >> 1) + 2 * (hg & 1) + wn) * 4096 + lane * 16;
  int vrest = a2b + (52 + 5 * (hg >> 1)) * 4096 + (2 * (hg & 1) + wn) * 256 + g * 1024 + tk * 16;
  PIN(ia); PIN(ib); PIN(ta); PIN(e4); PIN(vmain); PIN(vrest);
  STAMP(4);
  if (wave == 3) {
#pragma unroll
    for (int i = 0; i < KVDELAY; ++i) __builtin_amdgcn_s_sleep(8);
    wait_flag<KVSLEEP>(rs, (lane & 15) * 2048, OFF_F1 + hg * 128, tag);
    lds_arrive(lw + 56);
  } else {
#pragma unroll 1
    for (unsigned i = 0; i < (1u << 22); ++i) {
      unsigned x;
      asm volatile("s_sleep 1\n\tds_read_b32 %0, %1\n\ts_waitcnt lgkmcnt(0)" : "=v"(x) : "v"(lw + 56) : "memory");
      if (__builtin_amdgcn_readfirstlane(x) != 0) break;
    }
  }
  {
    f32x4 sa, sb;
    unsigned cnt;
    asm volatile(KV_ASM
                 : [sa] "=&v"(sa), [sb] "=&v"(sb), [cnt] "=&s"(cnt)
                 : [v0] "v"(tid < 210 ? OFF_KVP + hg * 16 * KVP_STRIDE + tid * 16 : WS_BYTES),
                   [ia] "v"(ia), [ib] "v"(ib), [rs] "s"(rs), [k0] "s"(2 * (bi & 7) * KVP_STRIDE)
                 : "memory", "vcc", "scc", KV_CLOBBERS);
    *(LAS v2u*)lp(ta) = pk4(sa);
    *(LAS v2u*)(lp(ta) + 512) = pk4(sb);
  }
  STAMP(6);
  lds_sync(lw + 8, 4);
  {
    auto op = [&](unsigned x, unsigned y) { return cat(*(LAS v2u*)lp(x), *(LAS v2u*)lp(y)); };
    const v4u Z0 = op(kd[5], kd[6]), Z1 = op(kd[7], kd[8]), K0 = op(kd[0], kd[1]), K1 = op(kd[2], kd[3]), K2 = op(kd[9], kd[4]);
    v4u RA[4], RB[4];
#pragma unroll
    for (int m = 0; m < 4; ++m) { RA[m] = op(ra[m][0], ra[m][1]); RB[m] = op(ra[m][2], ra[m][3]); }
    FENCE();
    const f32x4 z4 = {0.f, 0.f, 0.f, 0.f};
    f32x4 d0[4], d1[4], t0[4], t1[4], oa = z4, ob = z4;
#pragma unroll
    for (int m = 0; m < 4; ++m) { d0[m] = mfma32(Z0, q01[m], e4); d1[m] = mfma32(Z1, q12[m], e4); }
    FENCE();
    float s0[4], s1[4];
#pragma unroll
    for (int m = 0; m < 4; ++m) {
      t0[m] = mfma32(K0, q01[m], z4);
      t1[m] = mfma32(K2, q12[m], mfma32(K1, q01[m], z4));
      const float x0 = d0[m][0], x1 = d1[m][0];
      s0[m] = gv[m] * __builtin_amdgcn_rcpf(x0);
      s1[m] = gv[m] * __builtin_amdgcn_rcpf(x1);
    }
#pragma unroll
    for (int i = 0; i < 8; ++i) { __builtin_amdgcn_sched_group_barrier(0x008, 1, 0); __builtin_amdgcn_sched_group_barrier(0x002, 2, 0); }
    FENCE();
#pragma unroll
    for (int m = 0; m < 4; ++m) {
      oa = mfma32(RA[m], q12[m], oa);
      ob = mfma32(RB[m], q12[m], ob);
      st128(cat(pk4(mul4(t0[m], s0[m])), pk4(mul4(t1[m], g == 0 ? s0[m] : s1[m]))), rs, vmain, m * 1024);
    }
    const float sr = sel4(g, s1[0], s1[1], s1[2], s1[3]);
    st128(cat(pk4(mul4(oa, sr)), pk4(mul4(ob, sr))), rs, vrest, 0);
  }
  STAMP(7);
  publish(lw + 12, 4, rs, OFF_F2 + (r * 16 + 8 + hg) * 128, tag);
  STAMP(8);
  STAMP_OUT();
}
}

extern "C" void kernel_launch(void* const* d_in, const int* in_sizes, int n_in, void* d_out, int out_size, void* d_ws, size_t ws_size,
                              hipStream_t stream) {
  (void)in_sizes; (void)n_in; (void)out_size; (void)ws_size;
  (void)hipFuncSetAttribute((const void*)fused, hipFuncAttributeMaxDynamicSharedMemorySize, LDS_BYTES);
  Args a;
  a.x = (const char*)d_in[0]; a.wqkv = (const char*)d_in[1]; a.w1 = (const char*)d_in[4]; a.ws = (const char*)d_ws;
  a.w2 = (const char*)d_in[5]; a.wo = (const char*)d_in[2]; a.gw = (const char*)d_in[6]; a.out = (const char*)d_out;
  fused<<<256, 512, LDS_BYTES, stream>>>(a);
}
```
